```python
import torch

def _softmax(x, axis=-1):
    return torch.softmax(x, dim=axis)
def _gelu(x):
    return torch.nn.functional.gelu(x, approximate="tanh")
def _silu(x):
    return torch.nn.functional.silu(x)
def _sigmoid(x):
    return torch.sigmoid(x)
def _layernorm(x, axis=-1, eps=1e-5):
    m = x.mean(dim=axis, keepdim=True)
    v = x.var(dim=axis, keepdim=True, unbiased=False)
    return (x - m) / torch.sqrt(v + eps)
def _scatter_add(tgt, idx, src, axis=0):
    return tgt.clone().index_add_(axis, idx, src)
def _segment_sum(src, seg, n):
    out = torch.zeros((n,) + src.shape[1:], dtype=src.dtype)
    return out.index_add_(0, seg, src)
def _pad(x, pw):
    flat = tuple(v for pair in reversed(pw) for v in pair)
    return torch.nn.functional.pad(x, flat)

kernel_name = 'gated_mix__ssm_conv_h3__dense_activation_mish__prompt'

def setup_inputs():

    _w = (lambda a: (lambda t: t.to(torch.bfloat16) if t.is_floating_point() else t)(torch.as_tensor(a)))
    def _A_setup():
        B, S, D = 1, 2048, 448
        ks = 4
        return {
            "q": _w((lambda *s: torch.randn(*s, dtype=torch.bfloat16) * 0.05)(B, S, D)),
            "k": _w((lambda *s: torch.randn(*s, dtype=torch.bfloat16) * 0.4)(B, S, D)),
            "v": _w((lambda *s: torch.randn(*s, dtype=torch.bfloat16) * 0.2)(B, S, D)),
            "shift_k": _w((lambda *s: torch.randn(*s, dtype=torch.bfloat16) * 0.4)(D, ks) * 0.5),
            "ssm_k": _w((lambda *s: torch.randn(*s, dtype=torch.bfloat16) * 0.4)(D, S) * (1.0 / S ** 0.5)),
            "ssm_D": _w((lambda *s: torch.randn(*s, dtype=torch.bfloat16) * 0.2)(D)),
        }
    _A_in = _A_setup()

    def _B_setup():
        B, S, D, F = 1, 2048, 448, 1792
        return {
            "x": _w((lambda *s: torch.randn(*s, dtype=torch.bfloat16) * 0.05)(B, S, D)),
            "w1": _w((lambda *s: torch.randn(*s, dtype=torch.bfloat16) * 3.2)(D, F) * (D ** -0.5)),
            "b1": _w((lambda *s: torch.randn(*s, dtype=torch.bfloat16) * 0.05)(F)),
            "w2": _w((lambda *s: torch.randn(*s, dtype=torch.bfloat16) * 0.05)(F, D) * (F ** -0.5)),
        }
    _B_in = _B_setup()

    return {
        "A_q": _A_in["q"],
        "A_k": _A_in["k"],
        "A_v": _A_in["v"],
        "A_shift_k": _A_in["shift_k"],
        "A_ssm_k": _A_in["ssm_k"],
        "A_ssm_D": _A_in["ssm_D"],
        "B_x": _B_in["x"],
        "B_w1": _B_in["w1"],
        "B_b1": _B_in["b1"],
        "B_w2": _B_in["w2"],
        "gate_w": _w((lambda *s: torch.randn(*s, dtype=torch.bfloat16) * 0.05)(448) * 16.0),
    }

def reference(A_q, A_k, A_v, A_shift_k, A_ssm_k, A_ssm_D, B_x, B_w1, B_b1, B_w2, gate_w):
    def _A_ref(q, k, v, shift_k, ssm_k, ssm_D):
        B, S, D = q.shape
        ks = shift_k.shape[-1]
        k_pad = _pad(k, ((0, 0), (ks - 1, 0), (0, 0)))
        k_shift = torch.zeros_like(k)
        for t in range(ks):
            k_shift = k_shift + k_pad[:, t:t + S, :] * shift_k[:, ks - 1 - t][None, None, :]
        kv = k_shift * v
        L = 2 * S
        kv_pad = _pad(kv, ((0, 0), (0, S), (0, 0)))
        kf_pad = _pad(ssm_k, ((0, 0), (0, S)))
        kv_f = (lambda x, axes=(-2,-1): torch.fft.rfft2(x.float(), dim=axes))(kv_pad[:, None, :, :], axes=(1, 2))
        kf_f = (lambda x, axes=(-2,-1): torch.fft.rfft2(x.float(), dim=axes))(kf_pad[:, None, :], axes=(1, 2))
        kf_f = (lambda x, *d: x.permute(*d))(kf_f, 1, 2, 0)
        y_f = kv_f * kf_f[None, :, :, :]
        y = (lambda x, s, axes=(-2,-1): torch.fft.irfft2(x, s=s, dim=axes).to(torch.bfloat16))(y_f, (1, L), axes=(1, 2))[:, 0, :S, :]
        y = y + kv * ssm_D[None, None, :]
        return q * y

    def _B_ref(x, w1, b1, w2):
        h = torch.einsum("bsd,df->bsf", x, w1) + b1
        sp = torch.log1p(torch.exp(-torch.abs(h))) + torch.where(h > 0, h, torch.zeros_like(h))
        act = h * torch.tanh(sp)
        return torch.einsum("bsf,fd->bsd", act, w2)

    x = A_q
    g = _sigmoid((x * gate_w).sum(-1, keepdims=True))
    a = _A_ref(x, A_k, A_v, A_shift_k, A_ssm_k, A_ssm_D)
    b = _B_ref(x, B_w1, B_b1, B_w2)
    return g * a + (1 - g) * b
```

```cpp
#include <hip/hip_runtime.h>
#include <stdint.h>

typedef unsigned short u16;
typedef unsigned int u32;
typedef __attribute__((ext_vector_type(8))) short bf16x8;
typedef __attribute__((ext_vector_type(4))) short s16x4;
typedef __attribute__((ext_vector_type(4))) float f32x4;
typedef __attribute__((ext_vector_type(2))) float f32x2;
typedef __attribute__((ext_vector_type(4))) u32 u32x4;
typedef __attribute__((ext_vector_type(2))) u32 u32x2;
typedef __attribute__((ext_vector_type(2))) __bf16 bf16x2_t;
typedef __attribute__((address_space(3))) char* lds_ptr;

constexpr int S_ = 2048, D_ = 448, F_ = 1792;
constexpr size_t WS_ACT = 0;
constexpr size_t WS_W2T = 8u << 20;
constexpr size_t WS_UR  = 10u << 20;
constexpr size_t WS_YT  = 12u << 20;
constexpr size_t WS_G   = 14u << 20;

#define WAIT_V(n) asm volatile("s_waitcnt vmcnt(" #n ")" ::: "memory")
#define WAIT_L(n) asm volatile("s_waitcnt lgkmcnt(" #n ")" ::: "memory")
#define BAR() __builtin_amdgcn_s_barrier()

__device__ __forceinline__ float bf2f(u32 x) { return __uint_as_float(x << 16); }
__device__ __forceinline__ float bflo(u32 x) { return __uint_as_float(x << 16); }
__device__ __forceinline__ float bfhi(u32 x) { return __uint_as_float(x & 0xffff0000u); }
__device__ __forceinline__ u32 pk2(float lo, float hi) {
  f32x2 v = {lo, hi};
  return __builtin_bit_cast(u32, __builtin_convertvector(v, bf16x2_t));
}
__device__ __forceinline__ float rbf(float x) { return bflo(pk2(x, x)); }

__device__ __forceinline__ void glds16(const void* g, char* lds) {
  __builtin_amdgcn_global_load_lds((const u32*)g, (u32*)lds, 16, 0, 0);
}
__device__ __forceinline__ s16x4 trread(const char* p) {
  typedef __attribute__((ext_vector_type(4))) short v4i16_t;
  return __builtin_bit_cast(s16x4, __builtin_amdgcn_ds_read_tr16_b64_v4i16(
      (__attribute__((address_space(3))) v4i16_t*)(p)));
}
__device__ __forceinline__ bf16x8 cat8(s16x4 a, s16x4 b) {
  return __builtin_shufflevector(a, b, 0, 1, 2, 3, 4, 5, 6, 7);
}
__device__ __forceinline__ f32x4 mfma16(bf16x8 a, bf16x8 b, f32x4 c) {
  return __builtin_amdgcn_mfma_f32_16x16x32_bf16(a, b, c, 0, 0, 0);
}
__device__ __forceinline__ float mish_f(float h) {
  float t = __builtin_amdgcn_exp2f(fminf(h * 1.44269504088896341f, 30.0f));
  float n = t * (t + 2.0f);
  return h * n * __builtin_amdgcn_rcpf(n + 2.0f);
}

constexpr int G1_SLOT = 32768, G1_NSLOT = 4;
constexpr int PREP_OFF = G1_SLOT * G1_NSLOT;
constexpr int PREP_BLK = 1040;
constexpr int PREP_K = PREP_OFF, PREP_V = PREP_K + 9 * PREP_BLK, MISC_OFF = PREP_V + 8 * PREP_BLK;
constexpr int K0_LDS = MISC_OFF + 1024;

__global__ __launch_bounds__(512, 2) void k0_kernel(const u16* __restrict__ q, const u16* __restrict__ kin,
    const u16* __restrict__ vin, const u16* __restrict__ sk, const u16* __restrict__ w1,
    const u16* __restrict__ b1, const u16* __restrict__ w2, const u16* __restrict__ gw, char* __restrict__ ws) {
  __shared__ __attribute__((aligned(16))) char sm[K0_LDS];
  const int tid = threadIdx.x, lane = tid & 63;
  const int w = __builtin_amdgcn_readfirstlane(tid >> 6);
  const int wg = blockIdx.x;
  if (wg < 224) {
    const int xcd = wg & 7, idx = wg >> 3;
    const int rb = 2 * xcd + idx / 14, ft = idx % 14;
    const int sb = 4 * xcd + idx / 7, db = idx % 7;
    {
      const int r8 = lane >> 3, gq = lane & 7;
      const int s0 = 64 * sb;
      glds16(kin + (size_t)(s0 + 8 * w + r8) * D_ + 64 * db + 8 * gq, sm + PREP_K + (w + 1) * PREP_BLK);
      glds16(vin + (size_t)(s0 + 8 * w + r8) * D_ + 64 * db + 8 * gq, sm + PREP_V + w * PREP_BLK);
      if (w == 0) {
        int hr = s0 - 8 + r8; hr = hr < 0 ? 0 : hr;
        glds16(kin + (size_t)hr * D_ + 64 * db + 8 * gq, sm + PREP_K);
      } else if (w == 1) {
        const u16* p = (lane < 32) ? (sk + (size_t)(64 * db) * 4 + lane * 8) : (b1 + 128 * ft + (lane & 15) * 8);
        glds16(p, sm + MISC_OFF);
      }
    }
    const u16* xsrc0; const u16* wsrc0;
    {
      int row = 16 * w + (lane >> 3);
      int gr = (lane & 7) ^ ((row >> 1) & 7);
      xsrc0 = q + (size_t)(128 * rb + row) * D_ + gr * 8;
      int kr = 8 * w + (lane >> 4);
      int hh = (kr & 3) | (((kr >> 3) & 1) << 2);
      int gm = (lane & 15) ^ (2 * hh);
      wsrc0 = w1 + (size_t)kr * F_ + 128 * ft + gm * 8;
    }
    const u16* xsrc1 = q + (size_t)(128 * rb + 16 * w + 8 + (lane >> 3)) * D_ +
                       (((lane & 7) ^ (((16 * w + 8 + (lane >> 3)) >> 1) & 7)) * 8);
    auto stage = [&](int t) {
      char* slot = sm + (t & (G1_NSLOT - 1)) * G1_SLOT;
      glds16(xsrc0 + 64 * t, slot + (2 * w) * 1024);
      glds16(xsrc1 + 64 * t, slot + (2 * w + 1) * 1024);
      glds16(wsrc0 + (size_t)(64 * t) * F_, slot + 16384 + (2 * w) * 1024);
      glds16(wsrc0 + (size_t)(64 * t + 4) * F_, slot + 16384 + (2 * w + 1) * 1024);
    };
    stage(0); stage(1); stage(2); stage(3);
    WAIT_V(16);
    BAR();
    {
      const int rg = tid & 7, d = tid >> 3;
      const u16* kp = (const u16*)(sm + PREP_K + rg * PREP_BLK) + d;
      const u16* kp2 = (const u16*)(sm + PREP_K + (rg + 1) * PREP_BLK) + d;
      const u16* vp = (const u16*)(sm + PREP_V + rg * PREP_BLK) + d;
      u32x2 skv = *(const u32x2*)(sm + MISC_OFF + d * 8);
      float c0 = bflo(skv.x), c1 = bfhi(skv.x), c2 = bflo(skv.y), c3 = bfhi(skv.y);
      float kk[11];
      const bool first = (sb == 0) && (rg == 0);
#pragma unroll
      for (int i = 0; i < 3; ++i) kk[i] = first ? 0.f : bf2f(kp[(5 + i) * 64]);
#pragma unroll
      for (int i = 0; i < 8; ++i) kk[3 + i] = bf2f(kp2[i * 64]);
      float o[8];
#pragma unroll
      for (int i = 0; i < 8; ++i) {
        float a = rbf(kk[i] * c3);
        a = rbf(a + rbf(kk[i + 1] * c2));
        a = rbf(a + rbf(kk[i + 2] * c1));
        a = rbf(a + rbf(kk[i + 3] * c0));
        o[i] = a * bf2f(vp[i * 64]);
      }
      u32x4 r = {pk2(o[7], o[6]), pk2(o[5], o[4]), pk2(o[3], o[2]), pk2(o[1], o[0])};
      u16* ur = (u16*)(ws + WS_UR) + (size_t)(64 * db + d) * S_ + (2040 - 64 * sb - 8 * rg);
      *(u32x4*)ur = r;
    }
    const int wf = w & 3, wsb = w >> 2;
    const int c = lane & 15, g = lane >> 4, qq = (lane >> 2) & 3, pp = lane & 3;
    const int boff = (64 * wsb + c) * 128 + ((g ^ (c >> 1)) * 16);
    const int hh = qq | ((g & 1) << 2);
    const int aoff = 16384 + (8 * g + qq) * 256 + (((4 * wf + (pp >> 1)) ^ (2 * hh)) * 16) + 8 * (pp & 1);
    f32x4 acc[2][4];
    {
      const u32x2 b0 = *(const u32x2*)(sm + MISC_OFF + 512 + (32 * wf + 4 * g) * 2);
      const u32x2 b1v = *(const u32x2*)(sm + MISC_OFF + 512 + (32 * wf + 16 + 4 * g) * 2);
      (void)b0; (void)b1v;
#pragma unroll
      for (int st = 0; st < 4; ++st) { acc[0][st] = (f32x4){0, 0, 0, 0}; acc[1][st] = (f32x4){0, 0, 0, 0}; }
    }
#pragma unroll
    for (int t = 0; t < 7; ++t) {
      if (t == 0) WAIT_V(13); else if (t == 1) WAIT_V(9); else if (t == 2) WAIT_V(9); else if (t == 3) WAIT_V(9);
      else if (t == 4) WAIT_V(8); else if (t == 5) WAIT_V(4); else WAIT_V(0);
      BAR();
      if (t >= 1 && t + 3 < 7) stage(t + 3);
      const char* slot = sm + (t & (G1_NSLOT - 1)) * G1_SLOT;
#pragma unroll
      for (int ks = 0; ks < 2; ++ks) {
        bf16x8 bfr[4], afr[2];
#pragma unroll
        for (int st = 0; st < 4; ++st) bfr[st] = *(const bf16x8*)(slot + ((boff ^ (64 * ks)) + st * 2048));
#pragma unroll
        for (int T = 0; T < 2; ++T) {
          const char* ap = slot + ((aoff ^ (32 * T)) + ks * 8192);
          afr[T] = cat8(trread(ap), trread(ap + 1024));
        }
#pragma unroll
        for (int T = 0; T < 2; ++T)
#pragma unroll
          for (int st = 0; st < 4; ++st) acc[T][st] = mfma16(afr[T], bfr[st], acc[T][st]);
      }
      WAIT_L(0);
    }
    {
      const u32x2 b0 = *(const u32x2*)(sm + MISC_OFF + 512 + (32 * wf + 4 * g) * 2);
      const u32x2 b1v = *(const u32x2*)(sm + MISC_OFF + 512 + (32 * wf + 16 + 4 * g) * 2);
      float bb[8] = {bflo(b0.x), bfhi(b0.x), bflo(b0.y), bfhi(b0.y), bflo(b1v.x), bfhi(b1v.x), bflo(b1v.y), bfhi(b1v.y)};
      char* actp = ws + WS_ACT + ((size_t)(8 * rb + 4 * wsb) * 56 + (4 * ft + wf)) * 1024 + lane * 16;
#pragma unroll
      for (int st = 0; st < 4; ++st) {
        float m[8];
#pragma unroll
        for (int j = 0; j < 8; ++j) {
          float h = rbf(rbf(acc[j >> 2][st][j & 3]) + bb[j]);
          m[j] = mish_f(h);
        }
        u32x4 r = {pk2(m[0], m[1]), pk2(m[2], m[3]), pk2(m[4], m[5]), pk2(m[6], m[7])};
        *(u32x4*)(actp + (size_t)st * 56 * 1024) = r;
      }
    }
  } else {
    const int hb = wg - 224;
    for (int i = w; i < 56; i += 8) {
      int half = i / 28, j = i % 28;
      int kk = hb + 32 * half;
      if (kk < 56) glds16(w2 + (size_t)kk * 32 * D_ + j * 512 + lane * 8, sm + half * 28672 + j * 1024);
    }
    {
      u32x4 gv = {0, 0, 0, 0};
      if (lane < 56) gv = *(const u32x4*)(gw + lane * 8);
#pragma unroll
      for (int r = 0; r < 8; ++r) {
        int row = 64 * hb + 8 * w + r;
        u32x4 xv = {0, 0, 0, 0};
        if (lane < 56) xv = *(const u32x4*)(q + (size_t)row * D_ + lane * 8);
        float s = 0.f;
#pragma unroll
        for (int e = 0; e < 4; ++e) s += rbf(bflo(xv[e]) * bflo(gv[e])) + rbf(bfhi(xv[e]) * bfhi(gv[e]));
#pragma unroll
        for (int o = 32; o >= 1; o >>= 1) s += __shfl_xor(s, o);
        if (lane == 0) {
          float z = rbf(s);
          float gg = rbf(1.0f / (1.0f + __expf(-z)));
          ((float*)(ws + WS_G))[row] = gg;
        }
      }
    }
    WAIT_V(0);
    __syncthreads();
    const int g = lane >> 4, qq = (lane >> 2) & 3, pp = lane & 3;
    for (int i = w; i < 56; i += 8) {
      int half = i / 28, j = i % 28;
      int kk = hb + 32 * half;
      if (kk < 56) {
        int db = j >> 2, T = j & 3;
        const char* ap = sm + half * 28672 + (4 * g + qq) * 896 + (64 * db + 16 * pp + 4 * T) * 2;
        bf16x8 fr = cat8(trread(ap), trread(ap + 16 * 896));
        *(bf16x8*)(ws + WS_W2T + (((size_t)db * 56 + kk) * 4 + T) * 1024 + lane * 16) = fr;
      }
    }
  }
}

constexpr int CV_COPY = 272;
constexpr int CV_H = 2176 * 16;
constexpr int CV_U = 2304 * 2;
constexpr int CV_CH = CV_H + CV_U;
constexpr int CV_RED = 2 * CV_CH;
constexpr int K1_LDS = CV_RED + 16384;

__global__ __launch_bounds__(256, 1) void k1_kernel(const u16* __restrict__ ssmk, const u16* __restrict__ ssmD,
                                                    char* __restrict__ ws) {
  __shared__ __attribute__((aligned(16))) char sm[K1_LDS];
  const int tid = threadIdx.x, lane = tid & 63;
  const int w = __builtin_amdgcn_readfirstlane(tid >> 6);
  const int ch = w >> 1, half = w & 1;
  const int d = 2 * blockIdx.x + ch;
  char* hb = sm + ch * CV_CH;
  char* ub = hb + CV_H;
  const int t2 = tid & 127;
  {
    const u32x4* src = (const u32x4*)(ws + WS_UR + (size_t)d * 4096);
    u32x4 a = src[t2], b = src[t2 + 128];
    *(u32x4*)(ub + t2 * 16) = a;
    *(u32x4*)(ub + (t2 + 128) * 16) = b;
    if (t2 < 32) *(u32x4*)(ub + 4096 + t2 * 16) = (u32x4){0, 0, 0, 0};
  }
  {
    const u32x4* hs = (const u32x4*)(ssmk + (size_t)d * S_);
#pragma unroll
    for (int p = 0; p < 3; ++p) {
      int gam = (p < 2) ? (2 + t2 + 128 * p) : t2;
      if (p < 2 || t2 < 2) {
        u32x4 lo = {0, 0, 0, 0}, hi = {0, 0, 0, 0};
        if (gam >= 2) lo = hs[gam - 2];
        if (gam >= 1 && gam <= 256) hi = hs[gam - 1];
        u32 e[8] = {lo.x, lo.y, lo.z, lo.w, hi.x, hi.y, hi.z, hi.w};
        u32 o[7];
#pragma unroll
        for (int i = 0; i < 7; ++i) o[i] = __builtin_amdgcn_alignbit(e[i + 1], e[i], 16);
#pragma unroll
        for (int rho = 0; rho < 8; ++rho) {
          const int off = (0x6A2EF480u >> (4 * rho)) & 15;
          const int k0 = rho >> 1;
          u32x4 r = (rho & 1) ? (u32x4){o[k0], o[k0 + 1], o[k0 + 2], o[k0 + 3]}
                              : (u32x4){e[k0], e[k0 + 1], e[k0 + 2], e[k0 + 3]};
          *(u32x4*)(hb + (rho * CV_COPY + off + gam) * 16) = r;
        }
      }
    }
  }
  __syncthreads();
  const int b = lane & 15, g = lane >> 4;
  const int rho = (b + 1) & 7, beta = (b + 1) >> 3;
  const char* ap = hb + (rho * CV_COPY + ((0x6A2EF480u >> (4 * rho)) & 15) + g + beta) * 16 + half * 256;
  const char* bp = ub + (2048 - 16 * (b + 1) + 8 * g) * 2 + half * 256;
  f32x4 acc[8];
#pragma unroll
  for (int n = 0; n < 8; ++n) acc[n] = (f32x4){0, 0, 0, 0};
#pragma unroll
  for (int c = 0; c < 4; ++c) {
    bf16x8 af[8], bf[8];
#pragma unroll
    for (int i = 0; i < 8; ++i) {
      af[i] = *(const bf16x8*)(ap + (8 * i + c) * 64);
      bf[i] = *(const bf16x8*)(bp + c * 64 - i * 512);
    }
#pragma unroll
    for (int n = 0; n < 8; ++n)
#pragma unroll
      for (int i = 0; i <= n; ++i) acc[n] = mfma16(af[i], bf[n - i], acc[n]);
  }
  char* red = sm + CV_RED;
  f32x4 keep[4];
  if (half) {
#pragma unroll
    for (int j = 0; j < 4; ++j) { *(f32x4*)(red + w * 4096 + j * 1024 + lane * 16) = acc[j]; keep[j] = acc[4 + j]; }
  } else {
#pragma unroll
    for (int j = 0; j < 4; ++j) { *(f32x4*)(red + w * 4096 + j * 1024 + lane * 16) = acc[4 + j]; keep[j] = acc[j]; }
  }
  __syncthreads();
  const float Dv = bf2f(ssmD[d]);
#pragma unroll
  for (int j = 0; j < 4; ++j) {
    f32x4 o = *(const f32x4*)(red + (w ^ 1) * 4096 + j * 1024 + lane * 16);
    f32x4 m = keep[j];
    const int t = 256 * (4 * half + j) + 16 * b + 4 * g;
    u32x2 uu = *(const u32x2*)(ub + (2044 - t) * 2);
    float u0 = bfhi(uu.y), u1 = bflo(uu.y), u2 = bfhi(uu.x), u3 = bflo(uu.x);
    float y0 = rbf(m[0] + o[0]) + rbf(u0 * Dv);
    float y1 = rbf(m[1] + o[1]) + rbf(u1 * Dv);
    float y2 = rbf(m[2] + o[2]) + rbf(u2 * Dv);
    float y3 = rbf(m[3] + o[3]) + rbf(u3 * Dv);
    u32x2 r = {pk2(y0, y1), pk2(y2, y3)};
    *(u32x2*)(ws + WS_YT + ((size_t)d * S_ + t) * 2) = r;
  }
}

constexpr int K2_RED = 0;
constexpr int K2_Y = 65536;
constexpr int K2_LDS = K2_Y + 64 * 144;

__global__ __launch_bounds__(256, 1) void k2_kernel(const u16* __restrict__ q, const char* __restrict__ ws,
                                                    u16* __restrict__ out) {
  __shared__ __attribute__((aligned(16))) char sm[K2_LDS];
  const int tid = threadIdx.x, lane = tid & 63;
  const int w = __builtin_amdgcn_readfirstlane(tid >> 6);
  const int wg = blockIdx.x;
  const int xcd = wg & 7, idx = wg >> 3;
  const int sb = 4 * xcd + idx / 7, db = idx % 7;
  const char* ap = ws + WS_W2T + ((size_t)db * 56 + 14 * w) * 4096 + lane * 16;
  const char* bp = ws + WS_ACT + ((size_t)(4 * sb) * 56 + 14 * w) * 1024 + lane * 16;
  f32x4 acc[4][4];
#pragma unroll
  for (int i = 0; i < 4; ++i)
#pragma unroll
    for (int j = 0; j < 4; ++j) acc[i][j] = (f32x4){0, 0, 0, 0};
  constexpr int PF = 6;
  bf16x8 af[PF][4], bf[PF][4];
#pragma unroll
  for (int k = 0; k < PF; ++k)
#pragma unroll
    for (int i = 0; i < 4; ++i) {
      af[k][i] = *(const bf16x8*)(ap + k * 4096 + i * 1024);
      bf[k][i] = *(const bf16x8*)(bp + k * 1024 + (size_t)i * 56 * 1024);
    }
  u32x4 yv[2];
#pragma unroll
  for (int i = 0; i < 2; ++i) {
    int e = tid + 256 * i, dr = e >> 3, sc = e & 7;
    yv[i] = *(const u32x4*)(ws + WS_YT + ((size_t)(64 * db + dr) * S_ + 64 * sb + 8 * sc) * 2);
  }
  const int c = lane & 15, g = lane >> 4;
  const int srow = 64 * sb + 16 * w + c;
  const u32x4 q0 = *(const u32x4*)(q + (size_t)srow * D_ + 64 * db + 16 * g);
  const u32x4 q1 = *(const u32x4*)(q + (size_t)srow * D_ + 64 * db + 16 * g + 8);
  const float gg = ((const float*)(ws + WS_G))[srow];
  __builtin_amdgcn_sched_barrier(0);
#pragma unroll
  for (int k = 0; k < 14; ++k) {
#pragma unroll
    for (int T = 0; T < 4; ++T)
#pragma unroll
      for (int st = 0; st < 4; ++st) acc[T][st] = mfma16(af[k % PF][T], bf[k % PF][st], acc[T][st]);
    if (k + PF < 14) {
#pragma unroll
      for (int i = 0; i < 4; ++i) {
        af[k % PF][i] = *(const bf16x8*)(ap + (k + PF) * 4096 + i * 1024);
        bf[k % PF][i] = *(const bf16x8*)(bp + (k + PF) * 1024 + (size_t)i * 56 * 1024);
      }
    }
    __builtin_amdgcn_sched_barrier(0);
  }
#pragma unroll
  for (int i = 0; i < 2; ++i) {
    int e = tid + 256 * i, dr = e >> 3, sc = e & 7;
    *(u32x4*)(sm + K2_Y + dr * 144 + sc * 16) = yv[i];
  }
#pragma unroll
  for (int st = 0; st < 4; ++st)
#pragma unroll
    for (int T = 0; T < 4; ++T)
      *(f32x4*)(sm + K2_RED + w * 16384 + (st * 4 + T) * 1024 + lane * 16) = acc[T][st];
  __syncthreads();
  const int qq = (lane >> 2) & 3, pp = lane & 3;
  const float og = rbf(1.0f - gg);
  u32 res[8];
#pragma unroll
  for (int T = 0; T < 4; ++T) {
    f32x4 s = *(const f32x4*)(sm + K2_RED + (w * 4 + T) * 1024 + lane * 16);
#pragma unroll
    for (int o = 1; o < 4; ++o) s += *(const f32x4*)(sm + K2_RED + o * 16384 + (w * 4 + T) * 1024 + lane * 16);
    s16x4 yy = trread(sm + K2_Y + (16 * g + 4 * T + qq) * 144 + (16 * w + 4 * pp) * 2);
    u32x2 yu = __builtin_bit_cast(u32x2, yy);
    u32 qa = (T < 2) ? q0[2 * T] : q1[2 * T - 4], qb = (T < 2) ? q0[2 * T + 1] : q1[2 * T - 3];
    float a0 = rbf(bflo(qa) * bflo(yu.x)), a1 = rbf(bfhi(qa) * bfhi(yu.x));
    float a2 = rbf(bflo(qb) * bflo(yu.y)), a3 = rbf(bfhi(qb) * bfhi(yu.y));
    float o0 = rbf(gg * a0) + rbf(og * rbf(s[0]));
    float o1 = rbf(gg * a1) + rbf(og * rbf(s[1]));
    float o2 = rbf(gg * a2) + rbf(og * rbf(s[2]));
    float o3 = rbf(gg * a3) + rbf(og * rbf(s[3]));
    res[2 * T] = pk2(o0, o1);
    res[2 * T + 1] = pk2(o2, o3);
  }
  u16* op = out + (size_t)srow * D_ + 64 * db + 16 * g;
  *(u32x4*)op = (u32x4){res[0], res[1], res[2], res[3]};
  *(u32x4*)(op + 8) = (u32x4){res[4], res[5], res[6], res[7]};
}

extern "C" void kernel_launch(void* const* d_in, const int* in_sizes, int n_in, void* d_out, int out_size,
                              void* d_ws, size_t ws_size, hipStream_t stream) {
  const u16* q = (const u16*)d_in[0];
  const u16* k = (const u16*)d_in[1];
  const u16* v = (const u16*)d_in[2];
  const u16* sk = (const u16*)d_in[3];
  const u16* ssmk = (const u16*)d_in[4];
  const u16* ssmD = (const u16*)d_in[5];
  const u16* w1 = (const u16*)d_in[7];
  const u16* b1 = (const u16*)d_in[8];
  const u16* w2 = (const u16*)d_in[9];
  const u16* gw = (const u16*)d_in[10];
  char* ws = (char*)d_ws;
  k0_kernel<<<256, 512, 0, stream>>>(q, k, v, sk, w1, b1, w2, gw, ws);
  k1_kernel<<<224, 256, 0, stream>>>(ssmk, ssmD, ws);
  k2_kernel<<<224, 256, 0, stream>>>(q, ws, (u16*)d_out);
}
```

```cpp
#include <hip/hip_runtime.h>
#include <stdint.h>

typedef unsigned short u16;
typedef unsigned int u32;
typedef unsigned long long u64;
typedef __attribute__((ext_vector_type(8))) short bf16x8;
typedef __attribute__((ext_vector_type(4))) short s16x4;
typedef __attribute__((ext_vector_type(4))) float f32x4;
typedef __attribute__((ext_vector_type(2))) float f32x2;
typedef __attribute__((ext_vector_type(4))) u32 u32x4;
typedef __attribute__((ext_vector_type(2))) u32 u32x2;
typedef __attribute__((ext_vector_type(2))) __bf16 bf16x2_t;
typedef __attribute__((address_space(1))) u64 gu64;

constexpr int S_ = 2048, D_ = 448, F_ = 1792;
constexpr u32 WS_ACT = 0;
constexpr u32 WS_W2T = 8u << 20;
constexpr u32 WS_UR = 10u << 20;
constexpr u32 WS_YT = 14u << 20;
constexpr u32 WS_G = 18u << 20;
constexpr u32 WS_FLAG = 19u << 20;
constexpr u32 FL_AF = 1024, FL_HF = 4096;
constexpr u32 WS_END = 20u << 20;
constexpr u32 SPIN_MAX = 1u << 18;

#define WAIT_V(n) do { asm volatile("" ::: "memory"); __builtin_amdgcn_s_waitcnt(((n) & 15) | 0x0F70 | (((n) >> 4) << 14)); asm volatile("" ::: "memory"); } while (0)
#define WAIT_L(n) asm volatile("s_waitcnt lgkmcnt(" #n ")" ::: "memory")
#define BAR() do { asm volatile("" ::: "memory"); __builtin_amdgcn_s_barrier(); asm volatile("" ::: "memory"); } while (0)
#define RLX __ATOMIC_RELAXED, __HIP_MEMORY_SCOPE_AGENT
#define ST(i)

__device__ __forceinline__ float bflo(u32 x) { return __uint_as_float(x << 16); }
__device__ __forceinline__ float bfhi(u32 x) { return __uint_as_float(x & 0xffff0000u); }
__device__ __forceinline__ u32 pk2(float lo, float hi) {
  f32x2 v = {lo, hi};
  return __builtin_bit_cast(u32, __builtin_convertvector(v, bf16x2_t));
}
__device__ __forceinline__ float rbf(float x) { return bflo(pk2(x, x)); }

__device__ __forceinline__ void glds16(const void* g, char* lds) {
  __builtin_amdgcn_global_load_lds((const u32*)g, (u32*)lds, 16, 0, 0);
}
__device__ __forceinline__ s16x4 trread(const char* p) {
  typedef __attribute__((ext_vector_type(4))) short v4i16_t;
  return __builtin_bit_cast(s16x4, __builtin_amdgcn_ds_read_tr16_b64_v4i16(
      (__attribute__((address_space(3))) v4i16_t*)(p)));
}
__device__ __forceinline__ bf16x8 cat8(s16x4 a, s16x4 b) {
  return __builtin_shufflevector(a, b, 0, 1, 2, 3, 4, 5, 6, 7);
}
__device__ __forceinline__ f32x4 mfma16(bf16x8 a, bf16x8 b, f32x4 c) {
  return __builtin_amdgcn_mfma_f32_16x16x32_bf16(a, b, c, 0, 0, 0);
}
__device__ __forceinline__ float mish_f(float h) {
  float t = __builtin_amdgcn_exp2f(fminf(h * 1.44269504088896341f, 30.0f));
  float n = t * (t + 2.0f);
  return h * n * __builtin_amdgcn_rcpf(n + 2.0f);
}
#define DS_B128(dst, addr, off) asm volatile("ds_read_b128 %0, %1 offset:%2" : "=v"(dst) : "v"(addr), "i"(off) : "memory")
#define DS_B64(dst, addr, off) asm volatile("ds_read_b64 %0, %1 offset:%2" : "=v"(dst) : "v"(addr), "i"(off) : "memory")
#define DS_B32(dst, addr, off) asm volatile("ds_read_b32 %0, %1 offset:%2" : "=v"(dst) : "v"(addr), "i"(off) : "memory")
#define DS_U16(dst, addr, off) asm volatile("ds_read_u16 %0, %1 offset:%2" : "=v"(dst) : "v"(addr), "i"(off) : "memory")
#define DS_TR(dst, addr, off) asm volatile("ds_read_b64_tr_b16 %0, %1 offset:%2" : "=v"(dst) : "v"(addr), "i"(off) : "memory")

constexpr int G1_SLOT = 32768;
constexpr int PREP_BLK = 1040;
constexpr int PREP_K = 131072, PREP_V = PREP_K + 9 * PREP_BLK, MISC_OFF = PREP_V + 8 * PREP_BLK;
constexpr int LDS_TOTAL = MISC_OFF + 1024;
constexpr int CV_COPY = 272;
constexpr int CV_H = 2176 * 16;
constexpr int CV_U = 2304 * 2;
constexpr int CV_CH = CV_H + CV_U;
constexpr int CV_RED = 2 * CV_CH;
constexpr int Y_OFF = PREP_K;

__global__ __launch_bounds__(512, 2) void fused_kernel(const u16* __restrict__ q, const u16* __restrict__ kin,
    const u16* __restrict__ vin, const u16* __restrict__ sk, const u16* __restrict__ ssmk,
    const u16* __restrict__ ssmD, const u16* __restrict__ w1, const u16* __restrict__ b1,
    const u16* __restrict__ w2, const u16* __restrict__ gw, char* __restrict__ ws, u16* __restrict__ out) {
  __shared__ __attribute__((aligned(16))) char sm[LDS_TOTAL];
  const int tid = threadIdx.x, lane = tid & 63;
  const int w = __builtin_amdgcn_readfirstlane(tid >> 6);
  const int wg = blockIdx.x;
  ST(0);
  const auto rs = __builtin_amdgcn_make_buffer_rsrc(ws, 0, WS_END, 0x00020000);
  gu64* flags = (gu64*)(ws + WS_FLAG);
  u32x2 epoch;
  asm volatile("global_load_dwordx2 %0, %1, off sc1" : "=v"(epoch) : "v"(flags) : "memory");
  const u32 lbase = (u32)(uintptr_t)(__attribute__((address_space(3))) char*)sm;

  if (wg < 224) {
    const int xcd = wg & 7, idx = wg >> 3;
    const int i7 = idx / 7, m7 = idx - 7 * i7;
    const int rb = 4 * (xcd >> 1) + i7, ft = 7 * (xcd & 1) + m7;
    const int sb = 4 * xcd + i7, db = m7;
    const int cd = 2 * wg + (w >> 2);
    const int t8 = tid & 255;
    {
      const int r8 = lane >> 3, gq = lane & 7;
      const int s0 = 64 * sb;
      glds16(kin + (size_t)(s0 + 8 * w + r8) * D_ + 64 * db + 8 * gq, sm + PREP_K + (w + 1) * PREP_BLK);
      glds16(vin + (size_t)(s0 + 8 * w + r8) * D_ + 64 * db + 8 * gq, sm + PREP_V + w * PREP_BLK);
      int hr = s0 - 8 + r8; hr = hr < 0 ? 0 : hr;
      const u16* p = (w & 1) ? ((lane < 32) ? (sk + (size_t)(64 * db) * 4 + lane * 8) : (b1 + 128 * ft + (lane & 15) * 8))
                             : (kin + (size_t)hr * D_ + 64 * db + 8 * gq);
      if (w < 2) glds16(p, sm + ((w & 1) ? MISC_OFF : PREP_K));
    }
    const u16* xsrc0; const u16* xsrc1; const u16* wsrc0;
    {
      const int gr = (lane & 7) ^ (lane >> 4);
      xsrc0 = q + (size_t)(128 * rb + 16 * w + (lane >> 3)) * D_ + gr * 8;
      xsrc1 = q + (size_t)(128 * rb + 16 * w + 8 + (lane >> 3)) * D_ + (gr ^ 4) * 8;
      const int gm = (lane & 15) ^ (2 * (lane >> 4)) ^ (8 * (w & 1));
      wsrc0 = w1 + (size_t)(8 * w + (lane >> 4)) * F_ + 128 * ft + gm * 8;
    }
    auto stage = [&](int t) {
      char* slot = sm + (t & 3) * G1_SLOT;
      glds16(xsrc0 + 64 * t, slot + (2 * w) * 1024);
      glds16(xsrc1 + 64 * t, slot + (2 * w + 1) * 1024);
      glds16(wsrc0 + (size_t)(64 * t) * F_, slot + 16384 + (2 * w) * 1024);
      glds16(wsrc0 + (size_t)(64 * t + 4) * F_, slot + 16384 + (2 * w + 1) * 1024);
    };
    stage(0); stage(1); stage(2); stage(3);
    const u32x4* hs = (const u32x4*)(ssmk + (size_t)cd * S_);
    const u32x4 hlo = hs[t8];
    u32x4 hhi = hs[t8 < 255 ? t8 + 1 : 255];
    const u32 Dbits = ssmD[cd];
    asm volatile("s_waitcnt vmcnt(19)" : "+v"(epoch) :: "memory");
    BAR();
    ST(1);
    const u64 tag = (((u64)(u32)__builtin_amdgcn_readfirstlane(epoch.y) << 32) | (u32)__builtin_amdgcn_readfirstlane(epoch.x)) + 0x9E3779B97F4A7C15ull;
    const u32 tag32 = (u32)tag;
    {
      const int rg = tid & 7, d = tid >> 3;
      const u32 ka = lbase + PREP_K + rg * PREP_BLK + d * 2;
      u32 kr[11], vr[8]; u32x2 skv;
#pragma unroll
      for (int i = 0; i < 3; ++i) DS_U16(kr[i], ka, (5 + i) * 128);
#pragma unroll
      for (int i = 0; i < 8; ++i) DS_U16(kr[3 + i], ka, PREP_BLK + i * 128);
#pragma unroll
      for (int i = 0; i < 8; ++i) DS_U16(vr[i], ka, (PREP_V - PREP_K) + i * 128);
      const u32 sa = lbase + MISC_OFF + d * 8;
      DS_B64(skv, sa, 0);
      asm volatile("s_waitcnt lgkmcnt(0)" : "+v"(kr[0]), "+v"(kr[1]), "+v"(kr[2]), "+v"(kr[3]), "+v"(kr[4]), "+v"(kr[5]),
                   "+v"(kr[6]), "+v"(kr[7]), "+v"(kr[8]), "+v"(kr[9]), "+v"(kr[10]), "+v"(skv) :: "memory");
      asm volatile("" : "+v"(vr[0]), "+v"(vr[1]), "+v"(vr[2]), "+v"(vr[3]), "+v"(vr[4]), "+v"(vr[5]), "+v"(vr[6]), "+v"(vr[7]) :: "memory");
      const float c0 = bflo(skv.x), c1 = bfhi(skv.x), c2 = bflo(skv.y), c3 = bfhi(skv.y);
      const bool first = (sb == 0) && (rg == 0);
      float kk[11];
#pragma unroll
      for (int i = 0; i < 11; ++i) kk[i] = (i < 3 && first) ? 0.f : bflo(kr[i]);
      float o[8];
#pragma unroll
      for (int i = 0; i < 8; ++i)
        o[i] = (kk[i] * c3 + kk[i + 1] * c2 + kk[i + 2] * c1 + kk[i + 3] * c0) * bflo(vr[i]);
      const u32 uo = WS_UR + ((u32)(64 * db + d) * S_ + (2040 - 64 * sb - 8 * rg)) * 4;
      __builtin_amdgcn_raw_buffer_store_b128((u32x4){pk2(o[7], o[6]), tag32, pk2(o[5], o[4]), tag32}, rs, uo, 0, 16);
      __builtin_amdgcn_raw_buffer_store_b128((u32x4){pk2(o[3], o[2]), tag32, pk2(o[1], o[0]), tag32}, rs, uo + 16, 0, 16);
    }
    const int wf = w & 3, wsb = w >> 2;
    const int c = lane & 15, g = lane >> 4, qq = (lane >> 2) & 3, pp = lane & 3;
    f32x4 acc[2][4];
    {
      const u32 boff = lbase + (64 * wsb + c) * 128 + ((g ^ (c >> 1)) * 16);
      const int hh = qq | ((g & 1) << 2);
      const u32 aoff = lbase + 16384 + (8 * g + qq) * 256 + (((4 * wf + (pp >> 1)) ^ (2 * hh)) * 16) + 8 * (pp & 1);
      const u32 bA[2][2] = {{boff, boff ^ 64}, {boff + 65536, (boff ^ 64) + 65536}};
      const u32 aA[2][2] = {{aoff, aoff ^ 32}, {aoff + 65536, (aoff ^ 32) + 65536}};
      {
        u32x2 b0, b1v;
        const u32 ba = lbase + MISC_OFF + 512 + (32 * wf + 4 * g) * 2;
        DS_B64(b0, ba, 0); DS_B64(b1v, ba, 32);
        asm volatile("s_waitcnt lgkmcnt(0)" : "+v"(b0), "+v"(b1v) :: "memory");
#pragma unroll
        for (int st = 0; st < 4; ++st) {
          acc[0][st] = (f32x4){bflo(b0.x), bfhi(b0.x), bflo(b0.y), bfhi(b0.y)};
          acc[1][st] = (f32x4){bflo(b1v.x), bfhi(b1v.x), bflo(b1v.y), bfhi(b1v.y)};
        }
      }
#pragma unroll
      for (int t = 0; t < 7; ++t) {
        if (t == 0) WAIT_V(17); else if (t == 1) WAIT_V(13); else if (t == 2) WAIT_V(13); else if (t == 3) WAIT_V(13);
        else if (t == 4) WAIT_V(8); else if (t == 5) WAIT_V(4); else WAIT_V(0);
        BAR();
        if (t == 0) ST(2);
        if (t >= 1 && t + 3 < 7) stage(t + 3);
        const int hi = (t >> 1) & 1, so = (t & 1) * G1_SLOT;
        bf16x8 bfr[2][4]; s16x4 afr[2][2][2];
#pragma unroll
        for (int ks = 0; ks < 2; ++ks) {
#pragma unroll
          for (int T = 0; T < 2; ++T) {
            DS_TR(afr[ks][T][0], aA[hi][T], so + ks * 8192);
            DS_TR(afr[ks][T][1], aA[hi][T], so + ks * 8192 + 1024);
          }
#pragma unroll
          for (int st = 0; st < 4; ++st) DS_B128(bfr[ks][st], bA[hi][ks], so + st * 2048);
        }
#pragma unroll
        for (int ks = 0; ks < 2; ++ks) {
          if (ks == 0)
            asm volatile("s_waitcnt lgkmcnt(8)" : "+v"(afr[0][0][0]), "+v"(afr[0][0][1]), "+v"(afr[0][1][0]), "+v"(afr[0][1][1]),
                         "+v"(bfr[0][0]), "+v"(bfr[0][1]), "+v"(bfr[0][2]), "+v"(bfr[0][3]) :: "memory");
          else
            asm volatile("s_waitcnt lgkmcnt(0)" : "+v"(afr[1][0][0]), "+v"(afr[1][0][1]), "+v"(afr[1][1][0]), "+v"(afr[1][1][1]),
                         "+v"(bfr[1][0]), "+v"(bfr[1][1]), "+v"(bfr[1][2]), "+v"(bfr[1][3]) :: "memory");
          __builtin_amdgcn_sched_barrier(0);
#pragma unroll
          for (int T = 0; T < 2; ++T) {
            const bf16x8 a = cat8(afr[ks][T][0], afr[ks][T][1]);
#pragma unroll
            for (int st = 0; st < 4; ++st) acc[T][st] = mfma16(a, bfr[ks][st], acc[T][st]);
          }
          __builtin_amdgcn_sched_barrier(0);
        }
      }
    }
    ST(3);
    if (w == 0) {
      gu64* hf = flags + FL_HF / 8 + (lane & 31);
      for (u32 sp = 0; sp < SPIN_MAX; ++sp) {
        if (__all(__hip_atomic_load(hf, RLX) == tag)) break;
        __builtin_amdgcn_s_sleep(2);
      }
    }
    BAR();
    ST(4);
    const u32 uoff = WS_UR + (u32)cd * 8192 + t8 * 32;
    u32x4 ua = __builtin_amdgcn_raw_buffer_load_b128(rs, uoff, 0, 16);
    u32x4 ub4 = __builtin_amdgcn_raw_buffer_load_b128(rs, uoff + 16, 0, 16);
    bf16x8 af[7][4];
    const u32 wo = WS_W2T + (u32)(db * 56 + 7 * w) * 4096 + lane * 16;
#pragma unroll
    for (int k = 0; k < 4; ++k)
#pragma unroll
      for (int T = 0; T < 4; ++T)
        af[k][T] = __builtin_bit_cast(bf16x8, __builtin_amdgcn_raw_buffer_load_b128(rs, wo + k * 4096 + T * 1024, 0, 16));
    __builtin_amdgcn_sched_barrier(0);
    {
      const u32 ao = WS_ACT + ((u32)(8 * rb + 4 * wsb) * 56 + (4 * ft + wf)) * 1024 + lane * 16;
#pragma unroll
      for (int st = 0; st < 4; ++st) {
        float m[8];
#pragma unroll
        for (int j = 0; j < 8; ++j) m[j] = mish_f(acc[j >> 2][st][j & 3]);
        u32x4 r = {pk2(m[0], m[1]), pk2(m[2], m[3]), pk2(m[4], m[5]), pk2(m[6], m[7])};
        __builtin_amdgcn_raw_buffer_store_b128(r, rs, ao + st * 56 * 1024, 0, 16);
      }
    }
    ST(5);
    char* hb = sm + (w >> 2) * CV_CH;
    char* ub = hb + CV_H;
    {
      if (t8 == 255) hhi = (u32x4){0, 0, 0, 0};
#pragma unroll
      for (int p = 0; p < 2; ++p) {
        if (p == 0 || t8 < 2) {
          const int gam = p ? t8 : (t8 + 2);
          u32x4 lo = hlo, hi = hhi;
          if (p) {
            u32x4 h0;
#pragma unroll
            for (int e = 0; e < 4; ++e) h0[e] = __builtin_amdgcn_readfirstlane(hlo[e]);
            lo = (u32x4){0, 0, 0, 0};
            hi = t8 ? h0 : lo;
          }
          u32 e[8] = {lo.x, lo.y, lo.z, lo.w, hi.x, hi.y, hi.z, hi.w};
          u32 o[7];
#pragma unroll
          for (int i = 0; i < 7; ++i) o[i] = __builtin_amdgcn_alignbit(e[i + 1], e[i], 16);
#pragma unroll
          for (int rho = 0; rho < 8; ++rho) {
            const int off = (0x6A2EF480u >> (4 * rho)) & 15;
            const int k0 = rho >> 1;
            u32x4 r = (rho & 1) ? (u32x4){o[k0], o[k0 + 1], o[k0 + 2], o[k0 + 3]}
                                : (u32x4){e[k0], e[k0 + 1], e[k0 + 2], e[k0 + 3]};
            *(u32x4*)(hb + (rho * CV_COPY + off + gam) * 16) = r;
          }
        }
      }
      if (t8 < 32) *(u32x4*)(ub + 4096 + t8 * 16) = (u32x4){0, 0, 0, 0};
      for (u32 sp = 0; sp < SPIN_MAX; ++sp) {
        if (__all(ua.y == tag32 && ua.w == tag32 && ub4.y == tag32 && ub4.w == tag32)) break;
        __builtin_amdgcn_s_sleep(2);
        ua = __builtin_amdgcn_raw_buffer_load_b128(rs, uoff, 0, 16);
        ub4 = __builtin_amdgcn_raw_buffer_load_b128(rs, uoff + 16, 0, 16);
      }
      *(u32x4*)(ub + t8 * 16) = (u32x4){ua.x, ua.z, ub4.x, ub4.z};
    }
    ST(6);
    WAIT_V(0);
    WAIT_L(0);
    BAR();
    if (tid == 0) __hip_atomic_store(flags + FL_AF / 8 + rb * 16 + ft, tag, RLX);
    ST(7);
#pragma unroll
    for (int k = 4; k < 7; ++k)
#pragma unroll
      for (int T = 0; T < 4; ++T)
        af[k][T] = __builtin_bit_cast(bf16x8, __builtin_amdgcn_raw_buffer_load_b128(rs, wo + k * 4096 + T * 1024, 0, 16));
    __builtin_amdgcn_sched_barrier(0);
    {
      const int cq = w & 3;
      const int rho = (c + 1) & 7, beta = (c + 1) >> 3;
      const char* ap = hb + (rho * CV_COPY + ((0x6A2EF480u >> (4 * rho)) & 15) + g + beta) * 16 + cq * 128;
      const char* bp = ub + (2048 - 16 * (c + 1) + 8 * g) * 2 + cq * 128;
      f32x4 ca[8];
#pragma unroll
      for (int n = 0; n < 8; ++n) ca[n] = (f32x4){0, 0, 0, 0};
#pragma unroll
      for (int cl = 0; cl < 2; ++cl) {
        bf16x8 bf[8];
#pragma unroll
        for (int i = 0; i < 8; ++i) bf[i] = *(const bf16x8*)(bp + cl * 64 - i * 512);
#pragma unroll
        for (int i = 0; i < 8; ++i) {
          const bf16x8 a = *(const bf16x8*)(ap + (8 * i + cl) * 64);
#pragma unroll
          for (int n = i; n < 8; ++n) ca[n] = mfma16(a, bf[n - i], ca[n]);
        }
      }
      char* red = sm + CV_RED + (w >> 2) * 24576 + lane * 16;
      f32x4 k0, k1;
#pragma unroll
      for (int dq = 0; dq < 4; ++dq) {
        if (dq == cq) { k0 = ca[2 * dq]; k1 = ca[2 * dq + 1]; }
        else {
          const int sl = (cq - dq - 1) & 3;
          *(f32x4*)(red + (dq * 3 + sl) * 2048) = ca[2 * dq];
          *(f32x4*)(red + (dq * 3 + sl) * 2048 + 1024) = ca[2 * dq + 1];
        }
      }
      WAIT_L(0);
      BAR();
      ST(8);
#pragma unroll
      for (int sl = 0; sl < 3; ++sl) {
        k0 += *(const f32x4*)(red + (cq * 3 + sl) * 2048);
        k1 += *(const f32x4*)(red + (cq * 3 + sl) * 2048 + 1024);
      }
      const float Dv = bflo(Dbits);
#pragma unroll
      for (int j = 0; j < 2; ++j) {
        const f32x4 m = j ? k1 : k0;
        const int t = 256 * (2 * cq + j) + 16 * c + 4 * g;
        const u32x2 uu = *(const u32x2*)(ub + (2044 - t) * 2);
        const float y0 = m[0] + bfhi(uu.y) * Dv, y1 = m[1] + bflo(uu.y) * Dv;
        const float y2 = m[2] + bfhi(uu.x) * Dv, y3 = m[3] + bflo(uu.x) * Dv;
        __builtin_amdgcn_raw_buffer_store_b128((u32x4){pk2(y0, y1), tag32, pk2(y2, y3), tag32}, rs,
                                               WS_YT + ((u32)cd * S_ + t) * 4, 0, 16);
      }
    }
    if (w == 0) {
      gu64* afp = flags + FL_AF / 8 + (sb >> 1) * 16 + (lane < 14 ? lane : 0);
      for (u32 sp = 0; sp < SPIN_MAX; ++sp) {
        if (__all(__hip_atomic_load(afp, RLX) == tag)) break;
        __builtin_amdgcn_s_sleep(1);
      }
    }
    WAIT_L(0);
    BAR();
    ST(9);
    const int st2 = w >> 1, th = w & 1;
    const int srow = 64 * sb + 16 * st2 + c;
    u32x4 ya, yb; u32x2 gv;
    const u32 yo = WS_YT + ((u32)(64 * db + (tid >> 3)) * S_ + 64 * sb + 8 * (tid & 7)) * 4;
    {
      const u32 bo = WS_ACT + ((u32)(4 * sb) * 56 + 7 * w) * 1024 + lane * 16;
      bf16x8 bf[2][7];
#pragma unroll
      for (int st = 0; st < 2; ++st)
#pragma unroll
        for (int k = 0; k < 7; ++k)
          bf[st][k] = __builtin_bit_cast(bf16x8, __builtin_amdgcn_raw_buffer_load_b128(rs, bo + (st * 56 + k) * 1024, 0, 16));
      __builtin_amdgcn_sched_barrier(0);
#pragma unroll
      for (int st = 0; st < 4; ++st) {
        f32x4 ga[4];
#pragma unroll
        for (int T = 0; T < 4; ++T) ga[T] = (f32x4){0, 0, 0, 0};
#pragma unroll
        for (int k = 0; k < 7; ++k) {
#pragma unroll
          for (int T = 0; T < 4; ++T) ga[T] = mfma16(af[k][T], bf[st & 1][k], ga[T]);
          if (st < 2)
            bf[st & 1][k] = __builtin_bit_cast(bf16x8, __builtin_amdgcn_raw_buffer_load_b128(rs, bo + ((st + 2) * 56 + k) * 1024, 0, 16));
          __builtin_amdgcn_sched_barrier(0);
        }
        if (st == 1) {
          ya = __builtin_amdgcn_raw_buffer_load_b128(rs, yo, 0, 16);
          yb = __builtin_amdgcn_raw_buffer_load_b128(rs, yo + 16, 0, 16);
          gv = __builtin_amdgcn_raw_buffer_load_b64(rs, WS_G + srow * 8, 0, 16);
        }
#pragma unroll
        for (int T = 0; T < 4; ++T) *(f32x4*)(sm + w * 16384 + (st * 4 + T) * 1024 + lane * 16) = ga[T];
        __builtin_amdgcn_sched_barrier(0);
      }
    }
    ST(10);
    const u32x4 q0 = *(const u32x4*)(q + (size_t)srow * D_ + 64 * db + 32 * th + 8 * g);
    for (u32 sp = 0; sp < SPIN_MAX; ++sp) {
      if (__all(ya.y == tag32 && ya.w == tag32 && yb.y == tag32 && yb.w == tag32 && gv.y == tag32)) break;
      __builtin_amdgcn_s_sleep(1);
      ya = __builtin_amdgcn_raw_buffer_load_b128(rs, yo, 0, 16);
      yb = __builtin_amdgcn_raw_buffer_load_b128(rs, yo + 16, 0, 16);
      gv = __builtin_amdgcn_raw_buffer_load_b64(rs, WS_G + srow * 8, 0, 16);
    }
    *(u32x4*)(sm + Y_OFF + (tid >> 3) * 144 + (tid & 7) * 16) = (u32x4){ya.x, ya.z, yb.x, yb.z};
    WAIT_L(0);
    BAR();
    ST(11);
    {
      const float gg = __uint_as_float(gv.x), og = 1.0f - gg;
      u32 res[4];
#pragma unroll
      for (int T = 0; T < 2; ++T) {
        const char* rp = sm + (st2 * 4 + 2 * th + T) * 1024 + lane * 16;
        f32x4 s = *(const f32x4*)rp;
#pragma unroll
        for (int o = 1; o < 8; ++o) s += *(const f32x4*)(rp + o * 16384);
        const u32x2 yu = __builtin_bit_cast(u32x2, trread(sm + Y_OFF + (32 * th + 8 * g + 4 * T + qq) * 144 + (16 * st2 + 4 * pp) * 2));
        const u32 qa = q0[2 * T], qb = q0[2 * T + 1];
        const float o0 = gg * (bflo(qa) * bflo(yu.x)) + og * s[0];
        const float o1 = gg * (bfhi(qa) * bfhi(yu.x)) + og * s[1];
        const float o2 = gg * (bflo(qb) * bflo(yu.y)) + og * s[2];
        const float o3 = gg * (bfhi(qb) * bfhi(yu.y)) + og * s[3];
        res[2 * T] = pk2(o0, o1);
        res[2 * T + 1] = pk2(o2, o3);
      }
      *(u32x4*)(out + (size_t)srow * D_ + 64 * db + 32 * th + 8 * g) = (u32x4){res[0], res[1], res[2], res[3]};
    }
    ST(12);
  } else {
    const int hb = wg - 224;
#pragma unroll
    for (int ii = 0; ii < 7; ++ii) {
      const int i = w + 8 * ii, half = i / 28, j = i - 28 * half;
      const int kk = hb + 32 * half;
      if (kk < 56) glds16(w2 + (size_t)kk * 32 * D_ + j * 512 + lane * 8, sm + half * 28672 + j * 1024);
    }
    const int row0 = 64 * (4 * (hb & 7) + (hb >> 3)) + 8 * w;
    const int l56 = lane < 56 ? lane : 55;
    const u32x4 gwv = *(const u32x4*)(gw + l56 * 8);
    u32x4 xv[8];
#pragma unroll
    for (int r = 0; r < 8; ++r) xv[r] = *(const u32x4*)(q + (size_t)(row0 + r) * D_ + l56 * 8);
    WAIT_V(0);
    asm volatile("s_waitcnt vmcnt(0)" : "+v"(epoch) :: "memory");
    const u64 tag = (((u64)(u32)__builtin_amdgcn_readfirstlane(epoch.y) << 32) | (u32)__builtin_amdgcn_readfirstlane(epoch.x)) + 0x9E3779B97F4A7C15ull;
    BAR();
    const int g = lane >> 4, qq = (lane >> 2) & 3, pp = lane & 3;
#pragma unroll
    for (int ii = 0; ii < 7; ++ii) {
      const int i = w + 8 * ii, half = i / 28, j = i - 28 * half;
      const int kk = hb + 32 * half;
      if (kk < 56) {
        const int db = j >> 2, T = j & 3;
        const char* ap = sm + half * 28672 + (4 * g + qq) * 896 + (64 * db + 32 * (T >> 1) + 8 * pp + 4 * (T & 1)) * 2;
        const bf16x8 fr = cat8(trread(ap), trread(ap + 16 * 896));
        __builtin_amdgcn_raw_buffer_store_b128(__builtin_bit_cast(u32x4, fr), rs,
                                               WS_W2T + (((u32)db * 56 + kk) * 4 + T) * 1024 + lane * 16, 0, 16);
      }
    }
    float zs = 0.f;
#pragma unroll
    for (int r = 0; r < 8; ++r) {
      float s = 0.f;
#pragma unroll
      for (int e = 0; e < 4; ++e) s += bflo(xv[r][e]) * bflo(gwv[e]) + bfhi(xv[r][e]) * bfhi(gwv[e]);
      if (lane >= 56) s = 0.f;
#pragma unroll
      for (int o = 32; o >= 1; o >>= 1) s += __shfl_xor(s, o);
      if (lane == r) zs = s;
    }
    if (lane < 8) {
      const float gg = 1.0f / (1.0f + __expf(-zs));
      __builtin_amdgcn_raw_buffer_store_b64((u32x2){__float_as_uint(gg), (u32)tag}, rs, WS_G + (row0 + lane) * 8, 0, 16);
    }
    WAIT_V(0);
    BAR();
    if (tid == 0) __hip_atomic_store(flags + FL_HF / 8 + hb, tag, RLX);
    ST(1);
    if (hb == 0 && w == 0) {
      for (u32 sp = 0; sp < 4 * SPIN_MAX; ++sp) {
        bool ok = __hip_atomic_load(flags + FL_HF / 8 + (lane & 31), RLX) == tag;
#pragma unroll
        for (int j = 0; j < 4; ++j) {
          const int e = lane + 64 * j, f = (e & 15) < 14 ? e : (e & ~15);
          ok = ok && (__hip_atomic_load(flags + FL_AF / 8 + f, RLX) == tag);
        }
        if (__all(ok)) break;
        __builtin_amdgcn_s_sleep(8);
      }
      if (lane == 0) __hip_atomic_store(flags, tag, RLX);
    }
  }
}

extern "C" void kernel_launch(void* const* d_in, const int* in_sizes, int n_in, void* d_out, int out_size,
                              void* d_ws, size_t ws_size, hipStream_t stream) {
  fused_kernel<<<256, 512, 0, stream>>>((const u16*)d_in[0], (const u16*)d_in[1], (const u16*)d_in[2],
                                        (const u16*)d_in[3], (const u16*)d_in[4], (const u16*)d_in[5],
                                        (const u16*)d_in[7], (const u16*)d_in[8], (const u16*)d_in[9],
                                        (const u16*)d_in[10], (char*)d_ws, (u16*)d_out);
}
```

```cpp
#include <hip/hip_runtime.h>
#include <stdint.h>

typedef unsigned short u16;
typedef unsigned int u32;
typedef unsigned long long u64;
typedef __attribute__((ext_vector_type(8))) short bf16x8;
typedef __attribute__((ext_vector_type(4))) short s16x4;
typedef __attribute__((ext_vector_type(4))) float f32x4;
typedef __attribute__((ext_vector_type(2))) float f32x2;
typedef __attribute__((ext_vector_type(4))) u32 u32x4;
typedef __attribute__((ext_vector_type(2))) u32 u32x2;
typedef __attribute__((ext_vector_type(2))) __bf16 bf16x2_t;
typedef __attribute__((address_space(1))) u64 gu64;

constexpr int S_ = 2048, D_ = 448, F_ = 1792;
constexpr u32 WS_ACT = 0;
constexpr u32 WS_UR = 8u << 20;
constexpr u32 WS_YT = 12u << 20;
constexpr u32 WS_G = 16u << 20;
constexpr u32 WS_FLAG = WS_G + 65536;
constexpr u32 FL_HF = 1024, FL_AF = 4096;
constexpr u32 WS_END = 17u << 20;
constexpr u32 SPIN_MAX = 1u << 18;

#define WAIT_V(n) do { asm volatile("" ::: "memory"); __builtin_amdgcn_s_waitcnt(((n) & 15) | 0x0F70 | (((n) >> 4) << 14)); asm volatile("" ::: "memory"); } while (0)
#define WAIT_L(n) do { asm volatile("" ::: "memory"); __builtin_amdgcn_s_waitcnt(0xC07F | ((n) << 8)); asm volatile("" ::: "memory"); } while (0)
#define SB() __builtin_amdgcn_sched_barrier(0)
#define BAR() do { asm volatile("" ::: "memory"); __builtin_amdgcn_s_barrier(); asm volatile("" ::: "memory"); } while (0)
#define RLX __ATOMIC_RELAXED, __HIP_MEMORY_SCOPE_AGENT
#define ST(i)
#define STC(i)
#define ST_DUMP(n)

__device__ __forceinline__ float bflo(u32 x) { return __uint_as_float(x << 16); }
__device__ __forceinline__ float bfhi(u32 x) { return __uint_as_float(x & 0xffff0000u); }
__device__ __forceinline__ u32 pk2(float lo, float hi) {
  f32x2 v = {lo, hi};
  return __builtin_bit_cast(u32, __builtin_convertvector(v, bf16x2_t));
}
__device__ __forceinline__ float rbf(float x) { return bflo(pk2(x, x)); }

__device__ __forceinline__ void dma16(u32 voff, const void* sbase, u32 lds) {
  asm volatile("s_mov_b32 m0, %2\n\ts_nop 0\n\tglobal_load_lds_dwordx4 %0, %1" :: "v"(voff), "s"(sbase), "s"(lds) : "memory");
}
typedef __attribute__((address_space(3))) char lchar;
__device__ __forceinline__ s16x4 trread(const lchar* p) {
  typedef __attribute__((ext_vector_type(4))) short v4i16_t;
  return __builtin_bit_cast(s16x4, __builtin_amdgcn_ds_read_tr16_b64_v4i16(
      (__attribute__((address_space(3))) v4i16_t*)(p)));
}
__device__ __forceinline__ bf16x8 cat8(s16x4 a, s16x4 b) {
  return __builtin_shufflevector(a, b, 0, 1, 2, 3, 4, 5, 6, 7);
}
__device__ __forceinline__ f32x4 mfma16(bf16x8 a, bf16x8 b, f32x4 c) {
  return __builtin_amdgcn_mfma_f32_16x16x32_bf16(a, b, c, 0, 0, 0);
}
__device__ __forceinline__ float mish_f(float h) {
  float t = __builtin_amdgcn_exp2f(fminf(h * 1.44269504088896341f, 30.0f));
  float n = t * (t + 2.0f);
  return h * n * __builtin_amdgcn_rcpf(n + 2.0f);
}
#define DS_B128(dst, addr, off) asm volatile("ds_read_b128 %0, %1 offset:%2" : "=v"(dst) : "v"(addr), "i"(off) : "memory")
#define DS_B64(dst, addr, off) asm volatile("ds_read_b64 %0, %1 offset:%2" : "=v"(dst) : "v"(addr), "i"(off) : "memory")
#define DS_B32(dst, addr, off) asm volatile("ds_read_b32 %0, %1 offset:%2" : "=v"(dst) : "v"(addr), "i"(off) : "memory")
#define DS_U16(dst, addr, off) asm volatile("ds_read_u16 %0, %1 offset:%2" : "=v"(dst) : "v"(addr), "i"(off) : "memory")
#define DS_TR(dst, addr, off) asm volatile("ds_read_b64_tr_b16 %0, %1 offset:%2" : "=v"(dst) : "v"(addr), "i"(off) : "memory")

constexpr int G1_SLOT = 32768;
constexpr int CV_COPY = 4160;
constexpr int CV_H = 4 * CV_COPY;
constexpr int CV_U = 2304 * 2;
constexpr int CV_CH = CV_H + CV_U;
constexpr int CV_OFF = 3 * G1_SLOT;
constexpr int PREP_BLK = 1040;
constexpr int PREP_K = CV_OFF + 2 * CV_CH, PREP_V = PREP_K + 9 * PREP_BLK, MISC_OFF = PREP_V + 8 * PREP_BLK;
constexpr int LDS_TOTAL = MISC_OFF + 1024;
constexpr int Y_OFF = PREP_K;
typedef __attribute__((address_space(3))) const volatile u32x2 lds_vu2;
typedef __attribute__((address_space(3))) const u32x2 lds_u2;
typedef __attribute__((address_space(3))) u32x4 lds_u4;
typedef __attribute__((address_space(3))) f32x4 lds_f4;
typedef __attribute__((address_space(3))) const bf16x8 lds_b8;

__global__ __launch_bounds__(512, 2) void fused_kernel(const u16* __restrict__ q, const u16* __restrict__ kin,
    const u16* __restrict__ vin, const u16* __restrict__ sk, const u16* __restrict__ ssmk,
    const u16* __restrict__ ssmD, const u16* __restrict__ w1, const u16* __restrict__ b1,
    const u16* __restrict__ w2, const u16* __restrict__ gw, char* __restrict__ ws, u16* __restrict__ out) {
  __shared__ __attribute__((aligned(16))) char smraw[LDS_TOTAL];
  lchar* sm = (lchar*)smraw;
  const int tid = threadIdx.x, lane = tid & 63;
  const int w = __builtin_amdgcn_readfirstlane(tid >> 6);
  const int wg = blockIdx.x;
  ST(0); STC(14);
  const auto rs = __builtin_amdgcn_make_buffer_rsrc(ws, 0, WS_END, 0x00020000);
  gu64* flags = (gu64*)(ws + WS_FLAG);
  const u32 lbase = (u32)(uintptr_t)sm;
  u32x2 epoch;

  if (wg < 224) {
    const int xcd = wg & 7, idx = wg >> 3;
    const int i7 = idx / 7, m7 = idx - 7 * i7;
    const int rb = 4 * (xcd >> 1) + i7, ft = 7 * (xcd & 1) + m7;
    const int sb = 4 * xcd + i7, db = m7;
    const int cd = 2 * wg + (w >> 2);
    const int t8 = tid & 255;
    const u32 xo0 = (lane >> 3) * (D_ * 2) + ((lane & 7) ^ (lane >> 4)) * 16;
    const u32 xo1 = ((lane >> 3) + 8) * (D_ * 2) + ((lane & 7) ^ (lane >> 4) ^ 4) * 16;
    const u32 wo0 = (lane >> 4) * (F_ * 2) + ((lane & 15) ^ (2 * (lane >> 4)) ^ (8 * (w & 1))) * 16;
    const u16* xs = q + (size_t)(128 * rb + 16 * w) * D_;
    const u16* wsr = w1 + (size_t)(8 * w) * F_ + 128 * ft;
    auto stage = [&](int t, int p) {
      const u32 slot = lbase + (t % 3) * G1_SLOT + w * 2048;
      if (p == 0) dma16(xo0, xs + 64 * t, slot);
      if (p == 1) dma16(xo1, xs + 64 * t, slot + 1024);
      if (p == 2) dma16(wo0, wsr + (size_t)(64 * t) * F_, slot + 16384);
      if (p == 3) dma16(wo0, wsr + (size_t)(64 * t + 4) * F_, slot + 16384 + 1024);
    };
    const u32 w2o = (lane >> 3) * (D_ * 2) + ((lane & 7) ^ (2 * (lane >> 4))) * 16;
    const u16* w2s = w2 + (size_t)(224 * w) * D_ + 64 * db;
    auto w2piece = [&](int k, int p) {
      dma16(w2o, w2s + (size_t)(32 * k + 8 * p) * D_, lbase + ((k + 1) % 3) * G1_SLOT + w * 4096 + p * 1024);
    };
#pragma unroll
    for (int p = 0; p < 4; ++p) stage(0, p);
    asm volatile("global_load_dwordx2 %0, %1, off sc1" : "=v"(epoch) : "v"(flags) : "memory");
    {
      const u32 ko = (lane >> 3) * (D_ * 2) + (lane & 7) * 16;
      const size_t e0 = (size_t)(64 * sb + 8 * w) * D_ + 64 * db;
      dma16(ko, kin + e0, lbase + PREP_K + (w + 1) * PREP_BLK);
      dma16(ko, vin + e0, lbase + PREP_V + w * PREP_BLK);
      if (w == 0) dma16(ko, kin + (size_t)(sb ? 64 * sb - 8 : 0) * D_ + 64 * db, lbase + PREP_K);
      if (w == 1) {
        const u16* p = (lane < 32) ? (sk + 256 * db + lane * 8) : (b1 + 128 * ft + (lane & 15) * 8);
        asm volatile("s_mov_b32 m0, %1\n\ts_nop 0\n\tglobal_load_lds_dwordx4 %0, off" :: "v"(p), "s"(lbase + MISC_OFF) : "memory");
      }
    }
#pragma unroll
    for (int p = 0; p < 4; ++p) stage(1, p);
    u32x4 hlo; u32x2 hhi; u32 Dbits;
    {
      const u16* hs = ssmk + (size_t)cd * S_;
      asm volatile("global_load_dwordx4 %0, %1, %2" : "=v"(hlo) : "v"(t8 * 16), "s"(hs) : "memory");
      asm volatile("global_load_dwordx2 %0, %1, %2" : "=v"(hhi) : "v"(t8 < 255 ? t8 * 16 + 16 : 0), "s"(hs) : "memory");
      asm volatile("global_load_ushort %0, %1, %2" : "=v"(Dbits) : "v"(0), "s"(ssmD + cd) : "memory");
    }
#pragma unroll
    for (int p = 0; p < 4; ++p) stage(2, p);

    const int wf = w & 3, wsb = w >> 2;
    const int c = lane & 15, g = lane >> 4, qq = (lane >> 2) & 3, pp = lane & 3;
    lchar* hb = sm + CV_OFF + (w >> 2) * CV_CH;
    lchar* ub = hb + CV_H;
    const u32 uoff = WS_UR + (u32)cd * 8192 + t8 * 32;
    u32x4 ua, ub4;
    u64 tag; u32 tag32;
    f32x4 acc[2][4];
#pragma unroll
    for (int T = 0; T < 2; ++T)
#pragma unroll
      for (int st = 0; st < 4; ++st) acc[T][st] = (f32x4){0, 0, 0, 0};
    {
      const u32 boff = lbase + (64 * wsb + c) * 128 + ((g ^ (c >> 1)) * 16);
      const int hh = qq | ((g & 1) << 2);
      const u32 aoff = lbase + 16384 + (8 * g + qq) * 256 + (((4 * wf + (pp >> 1)) ^ (2 * hh)) * 16) + 8 * (pp & 1);
      const u32 bA[2][2] = {{boff, boff ^ 64}, {boff + 65536, (boff ^ 64) + 65536}};
      const u32 aA[2][2] = {{aoff, aoff ^ 32}, {aoff + 65536, (aoff ^ 32) + 65536}};
      bf16x8 bfr[2][4]; s16x4 afr[2][2][2];
      auto rd = [&](int h) {
        const int t = h >> 1, ks = h & 1, sl = t % 3, hi = sl == 2, so = (sl == 1) * G1_SLOT;
#pragma unroll
        for (int T = 0; T < 2; ++T) {
          DS_TR(afr[ks][T][0], aA[hi][T], so + ks * 8192);
          DS_TR(afr[ks][T][1], aA[hi][T], so + ks * 8192 + 1024);
        }
#pragma unroll
        for (int st = 0; st < 4; ++st) DS_B128(bfr[ks][st], bA[hi][ks], so + st * 2048);
      };
      WAIT_V(14);
      BAR();
      ST(1);
      rd(0);
#pragma unroll
      for (int h = 0; h < 14; ++h) {
        const int t = h >> 1, ks = h & 1;
        if (ks == 1) {
          if (h == 13) {
            asm volatile("s_waitcnt vmcnt(8)" : "+v"(ua), "+v"(ub4) :: "memory");
            for (u32 sp = 0; sp < SPIN_MAX; ++sp) {
              if (__all(ua.y == tag32 && ua.w == tag32 && ub4.y == tag32 && ub4.w == tag32)) break;
              __builtin_amdgcn_s_sleep(2);
              asm volatile("buffer_load_dwordx4 %0, %2, %3, 0 offen sc1\n\tbuffer_load_dwordx4 %1, %2, %3, 0 offen offset:16 sc1\n\ts_waitcnt vmcnt(0)"
                           : "=&v"(ua), "=&v"(ub4) : "v"(uoff), "s"(rs) : "memory");
            }
            *(lds_u4*)(ub + t8 * 16) = (u32x4){ua.x, ua.z, ub4.x, ub4.z};
          }
          asm volatile("s_waitcnt lgkmcnt(0)" : "+v"(afr[1][0][0]), "+v"(afr[1][0][1]), "+v"(afr[1][1][0]), "+v"(afr[1][1][1]),
                       "+v"(bfr[1][0]), "+v"(bfr[1][1]), "+v"(bfr[1][2]), "+v"(bfr[1][3]) :: "memory");
          if (h == 1) WAIT_V(7); else if (h == 3 || h == 5 || h == 11) WAIT_V(6); else if (h == 7 || h == 9) WAIT_V(4);
          BAR();
          if (h < 13) rd(h + 1);
          if (h == 9)
            asm volatile("buffer_load_dwordx4 %0, %2, %3, 0 offen sc1\n\tbuffer_load_dwordx4 %1, %2, %3, 0 offen offset:16 sc1"
                         : "=&v"(ua), "=&v"(ub4) : "v"(uoff), "s"(rs) : "memory");
        } else {
          rd(h + 1);
          asm volatile("s_waitcnt lgkmcnt(8)" : "+v"(afr[0][0][0]), "+v"(afr[0][0][1]), "+v"(afr[0][1][0]), "+v"(afr[0][1][1]),
                       "+v"(bfr[0][0]), "+v"(bfr[0][1]), "+v"(bfr[0][2]), "+v"(bfr[0][3]) :: "memory");
        }
        SB();
#pragma unroll
        for (int T = 0; T < 2; ++T) {
          const bf16x8 a = cat8(afr[ks][T][0], afr[ks][T][1]);
#pragma unroll
          for (int st = 0; st < 4; ++st) {
            acc[T][st] = mfma16(a, bfr[ks][st], acc[T][st]);
            if (ks == 1 && (st & 1) == 0) {
              SB();
              const int p = 2 * T + (st >> 1);
              if (t + 3 < 7) stage(t + 3, p); else w2piece(t - 4, p);
              SB();
            }
          }
        }
        SB();
        if (h == 2) {
          asm volatile("" : "+v"(epoch));
          tag = (((u64)(u32)__builtin_amdgcn_readfirstlane(epoch.y) << 32) | (u32)__builtin_amdgcn_readfirstlane(epoch.x)) + 0x9E3779B97F4A7C15ull;
          tag32 = (u32)tag;
          const int rg = tid & 7, d = tid >> 3;
          const u32 ka = lbase + PREP_K + rg * PREP_BLK + d * 2;
          u32 kr[11], vr[8]; u32x2 skv;
#pragma unroll
          for (int i = 0; i < 3; ++i) DS_U16(kr[i], ka, (5 + i) * 128);
#pragma unroll
          for (int i = 0; i < 8; ++i) DS_U16(kr[3 + i], ka, PREP_BLK + i * 128);
#pragma unroll
          for (int i = 0; i < 8; ++i) DS_U16(vr[i], ka, (PREP_V - PREP_K) + i * 128);
          const u32 sa = lbase + MISC_OFF + d * 8;
          DS_B64(skv, sa, 0);
          asm volatile("s_waitcnt lgkmcnt(0)" : "+v"(kr[0]), "+v"(kr[1]), "+v"(kr[2]), "+v"(kr[3]), "+v"(kr[4]), "+v"(kr[5]),
                       "+v"(kr[6]), "+v"(kr[7]), "+v"(kr[8]), "+v"(kr[9]), "+v"(kr[10]), "+v"(skv) :: "memory");
          asm volatile("" : "+v"(vr[0]), "+v"(vr[1]), "+v"(vr[2]), "+v"(vr[3]), "+v"(vr[4]), "+v"(vr[5]), "+v"(vr[6]), "+v"(vr[7]) :: "memory");
          const float c0 = bflo(skv.x), c1 = bfhi(skv.x), c2 = bflo(skv.y), c3 = bfhi(skv.y);
          const bool first = (sb == 0) && (rg == 0);
          float kk[11];
#pragma unroll
          for (int i = 0; i < 11; ++i) kk[i] = (i < 3 && first) ? 0.f : bflo(kr[i]);
          float o[8];
#pragma unroll
          for (int i = 0; i < 8; ++i)
            o[i] = (kk[i] * c3 + kk[i + 1] * c2 + kk[i + 2] * c1 + kk[i + 3] * c0) * bflo(vr[i]);
          const u32 uo = WS_UR + ((u32)(64 * db + d) * S_ + (2040 - 64 * sb - 8 * rg)) * 4;
          __builtin_amdgcn_raw_buffer_store_b128((u32x4){pk2(o[7], o[6]), tag32, pk2(o[5], o[4]), tag32}, rs, uo, 0, 16);
          __builtin_amdgcn_raw_buffer_store_b128((u32x4){pk2(o[3], o[2]), tag32, pk2(o[1], o[0]), tag32}, rs, uo + 16, 0, 16);
          SB();
          ST(2);
        }
        if (h == 4) {
          asm volatile("" : "+v"(hlo), "+v"(hhi), "+v"(Dbits));
          if (t8 == 255) hhi = (u32x2){0, 0};
          const u32 e[6] = {hlo.x, hlo.y, hlo.z, hlo.w, hhi.x, hhi.y};
          u32 o[5];
#pragma unroll
          for (int i = 0; i < 5; ++i) o[i] = __builtin_amdgcn_alignbit(e[i + 1], e[i], 16);
          lchar* hp = hb + (t8 + 2) * 16;
          *(lds_u4*)(hp) = hlo;
          *(lds_u4*)(hp + CV_COPY) = (u32x4){o[0], o[1], o[2], o[3]};
          *(lds_u4*)(hp + 2 * CV_COPY) = (u32x4){e[1], e[2], e[3], e[4]};
          *(lds_u4*)(hp + 3 * CV_COPY) = (u32x4){o[1], o[2], o[3], o[4]};
          if ((w & 3) == 0) {
            const u32 h0 = __builtin_amdgcn_readfirstlane(hlo.x), h1 = __builtin_amdgcn_readfirstlane(hlo.y);
            const int rho = lane >> 1;
            u32x4 z = {0, 0, 0, 0};
            if (lane == 3) z.w = h0 << 16;
            if (lane == 5) z.w = h0;
            if (lane == 7) { z.z = h0 << 16; z.w = __builtin_amdgcn_alignbit(h1, h0, 16); }
            lchar* zp = lane < 8 ? hb + rho * CV_COPY + (lane & 1) * 16 : ub + 4096 + (lane & 31) * 16;
            if (lane < 8 || lane >= 32) *(lds_u4*)zp = z;
          }
          SB();
        }
      }
    }
    ST(3);
    bf16x8 af[7][4];
    const lchar* tA[4];
    {
      const int sw = (2 * g + (qq >> 1)) & 3;
#pragma unroll
      for (int T = 0; T < 4; ++T) tA[T] = sm + w * 4096 + (4 * g + qq) * 128 + ((T ^ sw) * 32) + pp * 8;
    }
    auto w2frag = [&](int k) {
      const int so = ((k + 1) % 3) * G1_SLOT;
#pragma unroll
      for (int T = 0; T < 4; ++T) af[k][T] = cat8(trread(tA[T] + so), trread(tA[T] + so + 2048));
    };
    const int cq = w & 3;
    const lchar* ap = hb + ((c + 1) & 3) * CV_COPY + ((c + 1) >> 2) * 8 + g * 16 + cq * 128;
    const lchar* bp = ub + (2048 - 16 * (c + 1) + 8 * g) * 2 + cq * 128 - 3584;
    bf16x8 cb[8], cav[8];
    auto rdA = [&](int i, int cl) {
      const u32x2 lo = *(lds_vu2*)(ap + (8 * i + cl) * 64), hi = *(lds_vu2*)(ap + (8 * i + cl) * 64 + 8);
      return __builtin_bit_cast(bf16x8, (u32x4){lo.x, lo.y, hi.x, hi.y});
    };
    auto rdB = [&](int i, int cl) { return *(lds_b8*)(bp + cl * 64 + (7 - i) * 512); };
    {
      const u32x2 b0 = *(lds_u2*)(sm + MISC_OFF + 512 + (32 * wf + 4 * g) * 2);
      const u32x2 b1v = *(lds_u2*)(sm + MISC_OFF + 512 + (32 * wf + 4 * g) * 2 + 32);
      const float bias[8] = {bflo(b0.x), bfhi(b0.x), bflo(b0.y), bfhi(b0.y), bflo(b1v.x), bfhi(b1v.x), bflo(b1v.y), bfhi(b1v.y)};
      const u32 ao = WS_ACT + ((u32)(8 * rb + 4 * wsb) * 56 + (4 * ft + wf)) * 1024 + lane * 16;
#pragma unroll
      for (int st = 0; st < 4; ++st) {
        if (st == 0) WAIT_V(8); else if (st == 1) WAIT_V(9); else if (st == 2) WAIT_V(10);
        if (st < 3) w2frag(st);
        else {
          cav[0] = rdA(0, 0);
#pragma unroll
          for (int i = 0; i < 8; ++i) cb[i] = rdB(i, 0);
          cav[1] = rdA(1, 0);
        }
        SB();
        float m[8];
#pragma unroll
        for (int j = 0; j < 8; ++j) m[j] = mish_f(acc[j >> 2][st][j & 3] + bias[j]);
        u32x4 r = {pk2(m[0], m[1]), pk2(m[2], m[3]), pk2(m[4], m[5]), pk2(m[6], m[7])};
        __builtin_amdgcn_raw_buffer_store_b128(r, rs, ao + st * 56 * 1024, 0, 16);
        SB();
        if (st < 3) {
          WAIT_L(0);
#pragma unroll
          for (int p = 0; p < 4; ++p) w2piece(st + 3, p);
        }
        SB();
      }
    }
    ST(4);
    bf16x8 bf[2][7];
    const u32 bo = WS_ACT + ((u32)(4 * sb) * 56 + 7 * w) * 1024 + lane * 16;
    {
      f32x4 ca[8];
#pragma unroll
      for (int n = 0; n < 8; ++n) ca[n] = (f32x4){0, 0, 0, 0};
#pragma unroll
      for (int i = 0; i < 8; ++i) {
        if (i + 2 < 8) cav[i + 2] = rdA(i + 2, 0);
        if (i == 2) { WAIT_V(11); w2frag(3); }
        SB();
#pragma unroll
        for (int n = i; n < 8; ++n) ca[n] = mfma16(cav[i], cb[n - i], ca[n]);
        SB();
        cb[7 - i] = rdB(i, 1);
        if (i >= 6) cav[i - 6] = rdA(13 - i, 1);
        if (i == 3) {
          WAIT_L(0);
#pragma unroll
          for (int p = 0; p < 4; ++p) w2piece(6, p);
        }
        SB();
      }
#pragma unroll
      for (int j = 0; j < 8; ++j) {
        const int i = 7 - j;
        if (j + 2 < 8) cav[j + 2] = rdA(5 - j, 1);
        if (j == 4) {
          WAIT_V(4);
          if (lane == 0) __hip_atomic_store(flags + FL_AF / 8 + (2 * rb + wsb) * 56 + 4 * ft + wf, tag, RLX);
          ST(5);
          w2frag(4);
        }
        if (j == 5) w2frag(5);
        if (j == 7) { WAIT_V(1); w2frag(6); }
        SB();
#pragma unroll
        for (int n = i; n < 8; ++n) ca[n] = mfma16(cav[j], cb[7 - (n - i)], ca[n]);
        SB();
      }
      ST(6);
      WAIT_L(0);
      f32x4 k0, k1;
#pragma unroll
      for (int dq = 0; dq < 4; ++dq) {
        if (dq == cq) { k0 = ca[2 * dq]; k1 = ca[2 * dq + 1]; }
        else {
          lchar* red = sm + ((cq - dq - 1) & 3) * G1_SLOT + w * 4096 + lane * 16;
          *(lds_f4*)(red) = ca[2 * dq];
          *(lds_f4*)(red + 1024) = ca[2 * dq + 1];
        }
      }
      {
        gu64* afp = flags + FL_AF / 8 + sb * 56 + 7 * w + (lane < 7 ? lane : 0);
        for (u32 sp = 0; sp < SPIN_MAX; ++sp) {
          if (__all(__hip_atomic_load(afp, RLX) == tag)) break;
          __builtin_amdgcn_s_sleep(1);
        }
      }
      ST(7);
#pragma unroll
      for (int st = 0; st < 2; ++st)
#pragma unroll
        for (int k = 0; k < 7; ++k)
          bf[st][k] = __builtin_bit_cast(bf16x8, __builtin_amdgcn_raw_buffer_load_b128(rs, bo + (st * 56 + k) * 1024, 0, 16));
      WAIT_L(0);
      BAR();
      ST(8);
#pragma unroll
      for (int sl = 0; sl < 3; ++sl) {
        const lchar* red = sm + sl * G1_SLOT + ((w & 4) + ((cq + sl + 1) & 3)) * 4096 + lane * 16;
        k0 += *(const lds_f4*)(red);
        k1 += *(const lds_f4*)(red + 1024);
      }
      const float Dv = bflo(Dbits);
#pragma unroll
      for (int j = 0; j < 2; ++j) {
        const f32x4 m = j ? k1 : k0;
        const int t = 256 * (2 * cq + j) + 16 * c + 4 * g;
        const u32x2 uu = *(lds_u2*)(ub + (2044 - t) * 2);
        const float y0 = m[0] + bfhi(uu.y) * Dv, y1 = m[1] + bflo(uu.y) * Dv;
        const float y2 = m[2] + bfhi(uu.x) * Dv, y3 = m[3] + bflo(uu.x) * Dv;
        __builtin_amdgcn_raw_buffer_store_b128((u32x4){pk2(y0, y1), tag32, pk2(y2, y3), tag32}, rs,
                                               WS_YT + ((u32)cd * S_ + t) * 4, 0, 16);
      }
    }
    WAIT_L(0);
    BAR();
    ST(9);
    const int st2 = w >> 1, th = w & 1;
    const int srow = 64 * sb + 16 * st2 + c;
    u32x4 ya, yb, q0; u32x2 gv;
    const u32 yo = WS_YT + ((u32)(64 * db + (tid >> 3)) * S_ + 64 * sb + 8 * (tid & 7)) * 4;
    SB();
#pragma unroll
    for (int st = 0; st < 4; ++st) {
      f32x4 ga[4];
#pragma unroll
      for (int T = 0; T < 4; ++T) ga[T] = (f32x4){0, 0, 0, 0};
#pragma unroll
      for (int k = 0; k < 7; ++k) {
#pragma unroll
        for (int T = 0; T < 4; ++T) ga[T] = mfma16(af[k][T], bf[st & 1][k], ga[T]);
        if (st < 2)
          bf[st & 1][k] = __builtin_bit_cast(bf16x8, __builtin_amdgcn_raw_buffer_load_b128(rs, bo + ((st + 2) * 56 + k) * 1024, 0, 16));
        SB();
      }
      if (st == 2) {
        ya = __builtin_amdgcn_raw_buffer_load_b128(rs, yo, 0, 16);
        yb = __builtin_amdgcn_raw_buffer_load_b128(rs, yo + 16, 0, 16);
        gv = __builtin_amdgcn_raw_buffer_load_b64(rs, WS_G + srow * 8, 0, 16);
        q0 = *(const u32x4*)(q + (size_t)srow * D_ + 64 * db + 32 * th + 8 * g);
      }
#pragma unroll
      for (int T = 0; T < 4; ++T) *(lds_f4*)(sm + w * 16384 + (st * 4 + T) * 1024 + lane * 16) = ga[T];
      SB();
    }
    ST(10);
    for (u32 sp = 0; sp < SPIN_MAX; ++sp) {
      if (__all(ya.y == tag32 && ya.w == tag32 && yb.y == tag32 && yb.w == tag32 && gv.y == tag32)) break;
      __builtin_amdgcn_s_sleep(1);
      ya = __builtin_amdgcn_raw_buffer_load_b128(rs, yo, 0, 16);
      yb = __builtin_amdgcn_raw_buffer_load_b128(rs, yo + 16, 0, 16);
      gv = __builtin_amdgcn_raw_buffer_load_b64(rs, WS_G + srow * 8, 0, 16);
    }
    *(lds_u4*)(sm + Y_OFF + (tid >> 3) * 144 + (tid & 7) * 16) = (u32x4){ya.x, ya.z, yb.x, yb.z};
    WAIT_L(0);
    BAR();
    ST(11);
    {
      const float gg = __uint_as_float(gv.x), og = 1.0f - gg;
      u32 res[4];
#pragma unroll
      for (int u = 0; u < 2; ++u) {
        const lchar* rp = sm + (st2 * 4 + 2 * th + (g >> 1)) * 1024 + (c + 16 * (2 * (g & 1) + u)) * 16;
        f32x4 s = *(const lds_f4*)rp;
#pragma unroll
        for (int o = 1; o < 8; ++o) s += *(const lds_f4*)(rp + o * 16384);
        const u32x2 yu = __builtin_bit_cast(u32x2, trread(sm + Y_OFF + (32 * th + 8 * g + 4 * u + qq) * 144 + (16 * st2 + 4 * pp) * 2));
        const u32 qa = q0[2 * u], qb = q0[2 * u + 1];
        const float o0 = gg * (bflo(qa) * bflo(yu.x)) + og * s[0];
        const float o1 = gg * (bfhi(qa) * bfhi(yu.x)) + og * s[1];
        const float o2 = gg * (bflo(qb) * bflo(yu.y)) + og * s[2];
        const float o3 = gg * (bfhi(qb) * bfhi(yu.y)) + og * s[3];
        res[2 * u] = pk2(o0, o1);
        res[2 * u + 1] = pk2(o2, o3);
      }
      *(u32x4*)(out + (size_t)srow * D_ + 64 * db + 32 * th + 8 * g) = (u32x4){res[0], res[1], res[2], res[3]};
    }
    ST(12);
    ST_DUMP(13);
  } else {
    const int hb = wg - 224;
    asm volatile("global_load_dwordx2 %0, %1, off sc1" : "=v"(epoch) : "v"(flags) : "memory");
    const int row0 = 64 * (4 * (hb & 7) + (hb >> 3)) + 8 * w;
    const int l56 = lane < 56 ? lane : 55;
    const u32x4 gwv = *(const u32x4*)(gw + l56 * 8);
    u32x4 xv[8];
#pragma unroll
    for (int r = 0; r < 8; ++r) xv[r] = *(const u32x4*)(q + (size_t)(row0 + r) * D_ + l56 * 8);
    WAIT_V(0);
    asm volatile("" : "+v"(epoch) :: "memory");
    const u64 tag = (((u64)(u32)__builtin_amdgcn_readfirstlane(epoch.y) << 32) | (u32)__builtin_amdgcn_readfirstlane(epoch.x)) + 0x9E3779B97F4A7C15ull;
    BAR();
    if (tid == 0) __hip_atomic_store(flags + FL_HF / 8 + hb, tag, RLX);
    float zs = 0.f;
#pragma unroll
    for (int r = 0; r < 8; ++r) {
      float s = 0.f;
#pragma unroll
      for (int e = 0; e < 4; ++e) s += bflo(xv[r][e]) * bflo(gwv[e]) + bfhi(xv[r][e]) * bfhi(gwv[e]);
      if (lane >= 56) s = 0.f;
#pragma unroll
      for (int o = 32; o >= 1; o >>= 1) s += __shfl_xor(s, o);
      if (lane == r) zs = s;
    }
    if (lane < 8) {
      const float gg = 1.0f / (1.0f + __expf(-zs));
      __builtin_amdgcn_raw_buffer_store_b64((u32x2){__float_as_uint(gg), (u32)tag}, rs, WS_G + (row0 + lane) * 8, 0, 16);
    }
    ST(1);
    ST_DUMP(2);
    if (hb == 0 && w == 0) {
      for (u32 sp = 0; sp < 4 * SPIN_MAX; ++sp) {
        bool ok = __hip_atomic_load(flags + FL_HF / 8 + (lane & 31), RLX) == tag;
#pragma unroll
        for (int j = 0; j < 4; ++j) {
          const int e = lane + 64 * j, f = (e & 15) < 14 ? (e & 15) : 0;
          ok = ok && (__hip_atomic_load(flags + FL_AF / 8 + (e >> 4) * 112 + 4 * f, RLX) == tag);
        }
        if (__all(ok)) break;
        __builtin_amdgcn_s_sleep(16);
      }
      if (lane == 0) __hip_atomic_store(flags, tag, RLX);
    }
  }
}

extern "C" void kernel_launch(void* const* d_in, const int* in_sizes, int n_in, void* d_out, int out_size,
                              void* d_ws, size_t ws_size, hipStream_t stream) {
  fused_kernel<<<256, 512, 0, stream>>>((const u16*)d_in[0], (const u16*)d_in[1], (const u16*)d_in[2],
                                        (const u16*)d_in[3], (const u16*)d_in[4], (const u16*)d_in[5],
                                        (const u16*)d_in[7], (const u16*)d_in[8], (const u16*)d_in[9],
                                        (const u16*)d_in[10], (char*)d_ws, (u16*)d_out);
}
```

```cpp
#include <hip/hip_runtime.h>
#include <stdint.h>

typedef unsigned short u16;
typedef unsigned int u32;
typedef unsigned long long u64;
typedef __attribute__((ext_vector_type(8))) short bf16x8;
typedef __attribute__((ext_vector_type(4))) short s16x4;
typedef __attribute__((ext_vector_type(4))) float f32x4;
typedef __attribute__((ext_vector_type(2))) float f32x2;
typedef __attribute__((ext_vector_type(4))) u32 u32x4;
typedef __attribute__((ext_vector_type(2))) u32 u32x2;
typedef __attribute__((ext_vector_type(2))) __bf16 bf16x2_t;
typedef __attribute__((address_space(1))) u64 gu64;

constexpr int S_ = 2048, D_ = 448, F_ = 1792;
constexpr u32 WS_ACT = 0;
constexpr u32 WS_UR = 8u << 20;
constexpr u32 WS_YT = 12u << 20;
constexpr u32 WS_G = 16u << 20;
constexpr u32 WS_FLAG = WS_G + 65536;
constexpr u32 FL_HF = 1024, FL_AF = 4096;
constexpr u32 WS_END = 17u << 20;
constexpr u32 SPIN_MAX = 1u << 18;

#define WAIT_V(n) do { asm volatile("" ::: "memory"); __builtin_amdgcn_s_waitcnt(((n) & 15) | 0x0F70 | (((n) >> 4) << 14)); asm volatile("" ::: "memory"); } while (0)
#define WAIT_L(n) do { asm volatile("" ::: "memory"); __builtin_amdgcn_s_waitcnt(0xC07F | ((n) << 8)); asm volatile("" ::: "memory"); } while (0)
#define SB() __builtin_amdgcn_sched_barrier(0)
#define BAR() do { asm volatile("" ::: "memory"); __builtin_amdgcn_s_barrier(); asm volatile("" ::: "memory"); } while (0)
#define RLX __ATOMIC_RELAXED, __HIP_MEMORY_SCOPE_AGENT
#define ST(i)
#define STC(i)
#define ST_DUMP(n)

__device__ __forceinline__ float bflo(u32 x) { return __uint_as_float(x << 16); }
__device__ __forceinline__ float bfhi(u32 x) { return __uint_as_float(x & 0xffff0000u); }
__device__ __forceinline__ u32 pk2(float lo, float hi) {
  f32x2 v = {lo, hi};
  return __builtin_bit_cast(u32, __builtin_convertvector(v, bf16x2_t));
}
__device__ __forceinline__ float rbf(float x) { return bflo(pk2(x, x)); }

__device__ __forceinline__ void dma16(u32 voff, const void* sbase, u32 lds) {
  asm volatile("s_mov_b32 m0, %2\n\ts_nop 0\n\tglobal_load_lds_dwordx4 %0, %1" :: "v"(voff), "s"(sbase), "s"(lds) : "memory");
}
typedef __attribute__((address_space(3))) char lchar;
__device__ __forceinline__ s16x4 trread(const lchar* p) {
  typedef __attribute__((ext_vector_type(4))) short v4i16_t;
  return __builtin_bit_cast(s16x4, __builtin_amdgcn_ds_read_tr16_b64_v4i16(
      (__attribute__((address_space(3))) v4i16_t*)(p)));
}
__device__ __forceinline__ bf16x8 cat8(s16x4 a, s16x4 b) {
  return __builtin_shufflevector(a, b, 0, 1, 2, 3, 4, 5, 6, 7);
}
__device__ __forceinline__ f32x4 mfma16(bf16x8 a, bf16x8 b, f32x4 c) {
  return __builtin_amdgcn_mfma_f32_16x16x32_bf16(a, b, c, 0, 0, 0);
}
__device__ __forceinline__ u32x2 mish4(f32x4 h) {
  const f32x2 ha = {h[0], h[1]}, hb = {h[2], h[3]};
  const f32x2 za = ha * 1.44269504088896341f, zb = hb * 1.44269504088896341f;
  const f32x2 ta = {__builtin_amdgcn_exp2f(fminf(za[0], 15.0f)), __builtin_amdgcn_exp2f(fminf(za[1], 15.0f))};
  const f32x2 tb = {__builtin_amdgcn_exp2f(fminf(zb[0], 15.0f)), __builtin_amdgcn_exp2f(fminf(zb[1], 15.0f))};
  const f32x2 na = ta * (ta + 2.0f), nb = tb * (tb + 2.0f);
  const f32x2 da = na + 2.0f, db = nb + 2.0f;
  const f32x2 pp = da * db;
  const float R = __builtin_amdgcn_rcpf(pp[0] * pp[1]);
  const f32x2 rr = (f32x2){pp[1], pp[0]} * R;
  const f32x2 ma = ha * (na * (db * rr)), mb = hb * (nb * (da * rr));
  return (u32x2){pk2(ma[0], ma[1]), pk2(mb[0], mb[1])};
}
#define DS_B128(dst, addr, off) asm volatile("ds_read_b128 %0, %1 offset:%2" : "=v"(dst) : "v"(addr), "i"(off) : "memory")
#define DS_B64(dst, addr, off) asm volatile("ds_read_b64 %0, %1 offset:%2" : "=v"(dst) : "v"(addr), "i"(off) : "memory")
#define DS_B32(dst, addr, off) asm volatile("ds_read_b32 %0, %1 offset:%2" : "=v"(dst) : "v"(addr), "i"(off) : "memory")
#define DS_U16(dst, addr, off) asm volatile("ds_read_u16 %0, %1 offset:%2" : "=v"(dst) : "v"(addr), "i"(off) : "memory")
#define DS_TR(dst, addr, off) asm volatile("ds_read_b64_tr_b16 %0, %1 offset:%2" : "=v"(dst) : "v"(addr), "i"(off) : "memory")

constexpr int G1_SLOT = 32768;
constexpr int CV_COPY = 4160;
constexpr int CV_H = 4 * CV_COPY;
constexpr int CV_U = 2304 * 2;
constexpr int CV_CH = CV_H + CV_U;
constexpr int CV_OFF = 3 * G1_SLOT;
constexpr int PREP_BLK = 1040;
constexpr int PREP_K = CV_OFF + 2 * CV_CH, PREP_V = PREP_K + 9 * PREP_BLK, MISC_OFF = PREP_V + 8 * PREP_BLK;
constexpr int LDS_TOTAL = MISC_OFF + 1024;
constexpr int Y_OFF = PREP_K;
typedef __attribute__((address_space(3))) const volatile u32x2 lds_vu2;
typedef __attribute__((address_space(3))) const u32x2 lds_u2;
typedef __attribute__((address_space(3))) u32x4 lds_u4;
typedef __attribute__((address_space(3))) f32x4 lds_f4;
typedef __attribute__((address_space(3))) const bf16x8 lds_b8;

__global__ __launch_bounds__(512, 2) void fused_kernel(const u16* __restrict__ q, const u16* __restrict__ kin,
    const u16* __restrict__ vin, const u16* __restrict__ sk, const u16* __restrict__ ssmk,
    const u16* __restrict__ ssmD, const u16* __restrict__ w1, const u16* __restrict__ b1,
    const u16* __restrict__ w2, const u16* __restrict__ gw, char* __restrict__ ws, u16* __restrict__ out) {
  __shared__ __attribute__((aligned(16))) char smraw[LDS_TOTAL];
  lchar* sm = (lchar*)smraw;
  const int tid = threadIdx.x, lane = tid & 63;
  const int w = __builtin_amdgcn_readfirstlane(tid >> 6);
  const int wg = blockIdx.x;
  ST(0); STC(14);
  const auto rs = __builtin_amdgcn_make_buffer_rsrc(ws, 0, WS_END, 0x00020000);
  gu64* flags = (gu64*)(ws + WS_FLAG);
  const u32 lbase = (u32)(uintptr_t)sm;
  u32x2 epoch;

  if (wg < 224) {
    const int xcd = wg & 7, idx = wg >> 3;
    const int i7 = idx / 7, m7 = idx - 7 * i7;
    const int rb = 4 * (xcd >> 1) + i7, ft = 7 * (xcd & 1) + m7;
    const int sb = 4 * xcd + i7, db = m7;
    const int cd = 2 * wg + (w >> 2);
    const int t8 = tid & 255;
    const u32 xo0 = (lane >> 3) * (D_ * 2) + ((lane & 7) ^ (lane >> 4)) * 16;
    const u32 xo1 = ((lane >> 3) + 8) * (D_ * 2) + ((lane & 7) ^ (lane >> 4) ^ 4) * 16;
    const u32 wo0 = (lane >> 4) * (F_ * 2) + ((lane & 15) ^ (2 * (lane >> 4)) ^ (8 * (w & 1))) * 16;
    const u16* xs = q + (size_t)(128 * rb + 16 * w) * D_;
    const u16* wsr = w1 + (size_t)(8 * w) * F_ + 128 * ft;
    auto stage = [&](int t, int p) {
      const u32 slot = lbase + (t % 3) * G1_SLOT + w * 2048;
      if (p == 0) dma16(xo0, xs + 64 * t, slot);
      if (p == 1) dma16(xo1, xs + 64 * t, slot + 1024);
      if (p == 2) dma16(wo0, wsr + (size_t)(64 * t) * F_, slot + 16384);
      if (p == 3) dma16(wo0, wsr + (size_t)(64 * t + 4) * F_, slot + 16384 + 1024);
    };
    const u32 w2o = (lane >> 3) * (D_ * 2) + ((lane & 7) ^ (2 * (lane >> 4))) * 16;
    const u16* w2s = w2 + (size_t)(224 * w) * D_ + 64 * db;
    auto w2piece = [&](int k, int p) {
      dma16(w2o, w2s + (size_t)(32 * k + 8 * p) * D_, lbase + ((k + 1) % 3) * G1_SLOT + w * 4096 + p * 1024);
    };
#pragma unroll
    for (int p = 0; p < 4; ++p) stage(0, p);
    asm volatile("global_load_dwordx2 %0, %1, off sc1" : "=v"(epoch) : "v"(flags) : "memory");
    {
      const u32 ko = (lane >> 3) * (D_ * 2) + (lane & 7) * 16;
      const size_t e0 = (size_t)(64 * sb + 8 * w) * D_ + 64 * db;
      dma16(ko, kin + e0, lbase + PREP_K + (w + 1) * PREP_BLK);
      dma16(ko, vin + e0, lbase + PREP_V + w * PREP_BLK);
      if (w == 0) dma16(ko, kin + (size_t)(sb ? 64 * sb - 8 : 0) * D_ + 64 * db, lbase + PREP_K);
      if (w == 1) {
        const u16* p = (lane < 32) ? (sk + 256 * db + lane * 8) : (b1 + 128 * ft + (lane & 15) * 8);
        asm volatile("s_mov_b32 m0, %1\n\ts_nop 0\n\tglobal_load_lds_dwordx4 %0, off" :: "v"(p), "s"(lbase + MISC_OFF) : "memory");
      }
    }
#pragma unroll
    for (int p = 0; p < 4; ++p) stage(1, p);
    u32 touch;
    {
      int xi = m7 * 147 + (t8 < 147 ? t8 : 146); xi = xi < 1024 ? xi : 1023;
      const u32 to = (w < 4) ? (u32)(192 + 64 * i7 + (t8 >> 2)) * (F_ * 2) + ft * 256 + (t8 & 3) * 64
                             : (u32)(128 * rb + (xi >> 3)) * (D_ * 2) + 384 + (xi & 7) * 64;
      asm volatile("global_load_dword %0, %1, %2" : "=v"(touch) : "v"(to), "s"(w < 4 ? w1 : q) : "memory");
    }
    u32x4 hlo; u32x2 hhi; u32 Dbits;
    const u16* hs = ssmk + (size_t)cd * S_;

    const int wf = w & 3, wsb = w >> 2;
    const int c = lane & 15, g = lane >> 4, qq = (lane >> 2) & 3, pp = lane & 3;
    lchar* hb = sm + CV_OFF + (w >> 2) * CV_CH;
    lchar* ub = hb + CV_H;
    const u32 uoff = WS_UR + (u32)cd * 8192 + t8 * 32;
    u32x4 ua, ub4;
    u64 tag; u32 tag32;
    f32x4 acc[2][4];
#pragma unroll
    for (int T = 0; T < 2; ++T)
#pragma unroll
      for (int st = 0; st < 4; ++st) acc[T][st] = (f32x4){0, 0, 0, 0};
    {
      const u32 boff = lbase + (64 * wsb + c) * 128 + ((g ^ (c >> 1)) * 16);
      const int hh = qq | ((g & 1) << 2);
      const u32 aoff = lbase + 16384 + (8 * g + qq) * 256 + (((4 * wf + (pp >> 1)) ^ (2 * hh)) * 16) + 8 * (pp & 1);
      const u32 bA[2][2] = {{boff, boff ^ 64}, {boff + 65536, (boff ^ 64) + 65536}};
      const u32 aA[2][2] = {{aoff, aoff ^ 32}, {aoff + 65536, (aoff ^ 32) + 65536}};
      bf16x8 bfr[2][4]; s16x4 afr[2][2][2];
      auto rd = [&](int h) {
        const int t = h >> 1, ks = h & 1, sl = t % 3, hi = sl == 2, so = (sl == 1) * G1_SLOT;
#pragma unroll
        for (int T = 0; T < 2; ++T) {
          DS_TR(afr[ks][T][0], aA[hi][T], so + ks * 8192);
          DS_TR(afr[ks][T][1], aA[hi][T], so + ks * 8192 + 1024);
        }
#pragma unroll
        for (int st = 0; st < 4; ++st) DS_B128(bfr[ks][st], bA[hi][ks], so + st * 2048);
      };
      WAIT_V(8);
      BAR();
      ST(1);
      rd(0);
#pragma unroll
      for (int h = 0; h < 14; ++h) {
        const int t = h >> 1, ks = h & 1;
        if (ks == 1) {
          if (h == 13) {
            asm volatile("s_waitcnt vmcnt(8)" : "+v"(ua), "+v"(ub4) :: "memory");
            for (u32 sp = 0; sp < SPIN_MAX; ++sp) {
              if (__all(ua.y == tag32 && ua.w == tag32 && ub4.y == tag32 && ub4.w == tag32)) break;
              __builtin_amdgcn_s_sleep(2);
              asm volatile("buffer_load_dwordx4 %0, %2, %3, 0 offen sc1\n\tbuffer_load_dwordx4 %1, %2, %3, 0 offen offset:16 sc1\n\ts_waitcnt vmcnt(0)"
                           : "=&v"(ua), "=&v"(ub4) : "v"(uoff), "s"(rs) : "memory");
            }
            *(lds_u4*)(ub + t8 * 16) = (u32x4){ua.x, ua.z, ub4.x, ub4.z};
          }
          asm volatile("s_waitcnt lgkmcnt(0)" : "+v"(afr[1][0][0]), "+v"(afr[1][0][1]), "+v"(afr[1][1][0]), "+v"(afr[1][1][1]),
                       "+v"(bfr[1][0]), "+v"(bfr[1][1]), "+v"(bfr[1][2]), "+v"(bfr[1][3]) :: "memory");
          if (h == 1) WAIT_V(8); else if (h == 3 || h == 5 || h == 11) WAIT_V(6); else if (h == 7 || h == 9) WAIT_V(4);
          BAR();
          if (h < 13) rd(h + 1);
          if (h == 9)
            asm volatile("buffer_load_dwordx4 %0, %2, %3, 0 offen sc1\n\tbuffer_load_dwordx4 %1, %2, %3, 0 offen offset:16 sc1"
                         : "=&v"(ua), "=&v"(ub4) : "v"(uoff), "s"(rs) : "memory");
        } else {
          rd(h + 1);
          asm volatile("s_waitcnt lgkmcnt(8)" : "+v"(afr[0][0][0]), "+v"(afr[0][0][1]), "+v"(afr[0][1][0]), "+v"(afr[0][1][1]),
                       "+v"(bfr[0][0]), "+v"(bfr[0][1]), "+v"(bfr[0][2]), "+v"(bfr[0][3]) :: "memory");
        }
        SB();
#pragma unroll
        for (int T = 0; T < 2; ++T) {
          const bf16x8 a = cat8(afr[ks][T][0], afr[ks][T][1]);
#pragma unroll
          for (int st = 0; st < 4; ++st) {
            acc[T][st] = mfma16(a, bfr[ks][st], acc[T][st]);
            if (ks == 1 && (st & 1) == 0) {
              SB();
              const int p = 2 * T + (st >> 1);
              if (t + 3 < 7) stage(t + 3, p); else w2piece(t - 4, p);
              SB();
            }
            if (h == 0) {
              SB();
              if (T == 0) stage(2, st);
              else if (st == 0) asm volatile("global_load_dwordx4 %0, %1, %2" : "=v"(hlo) : "v"(t8 * 16), "s"(hs) : "memory");
              else if (st == 1) asm volatile("global_load_dwordx2 %0, %1, %2" : "=v"(hhi) : "v"(t8 < 255 ? t8 * 16 + 16 : 0), "s"(hs) : "memory");
              else if (st == 2) asm volatile("global_load_ushort %0, %1, %2" : "=v"(Dbits) : "v"(0), "s"(ssmD + cd) : "memory");
              SB();
            }
          }
        }
        SB();
        if (h == 2) {
          asm volatile("" : "+v"(epoch));
          tag = (((u64)(u32)__builtin_amdgcn_readfirstlane(epoch.y) << 32) | (u32)__builtin_amdgcn_readfirstlane(epoch.x)) + 0x9E3779B97F4A7C15ull;
          tag32 = (u32)tag;
          const int r4 = tid & 15, dd = tid >> 4;
          const u32 kb = lbase + PREP_K + (1 + (r4 >> 1)) * PREP_BLK + (r4 & 1) * 512 + dd * 4;
          const u32 ka = kb - ((r4 & 1) ? 384 : 400);
          u32 kr[7], vr[4]; u32x4 skv;
#pragma unroll
          for (int i = 0; i < 3; ++i) DS_B32(kr[i], ka, i * 128);
#pragma unroll
          for (int i = 0; i < 4; ++i) DS_B32(kr[3 + i], kb, i * 128);
#pragma unroll
          for (int i = 0; i < 4; ++i) DS_B32(vr[i], kb, (PREP_V - PREP_K - PREP_BLK) + i * 128);
          const u32 sa = lbase + MISC_OFF + dd * 16;
          DS_B128(skv, sa, 0);
          asm volatile("s_waitcnt lgkmcnt(0)" : "+v"(kr[0]), "+v"(kr[1]), "+v"(kr[2]), "+v"(kr[3]), "+v"(kr[4]), "+v"(kr[5]),
                       "+v"(kr[6]), "+v"(vr[0]), "+v"(vr[1]), "+v"(vr[2]), "+v"(vr[3]), "+v"(skv) :: "memory");
          if (sb == 0 && r4 == 0) { kr[0] = 0; kr[1] = 0; kr[2] = 0; }
          const u32 uo = WS_UR + ((u32)(64 * db + 2 * dd) * S_ + (2044 - 64 * sb - 4 * r4)) * 4;
#pragma unroll
          for (int e = 0; e < 2; ++e) {
            const float c0 = bflo(skv[2 * e]), c1 = bfhi(skv[2 * e]), c2 = bflo(skv[2 * e + 1]), c3 = bfhi(skv[2 * e + 1]);
            float kk[7], o[4];
#pragma unroll
            for (int i = 0; i < 7; ++i) kk[i] = e ? bfhi(kr[i]) : bflo(kr[i]);
#pragma unroll
            for (int i = 0; i < 4; ++i)
              o[i] = (kk[i] * c3 + kk[i + 1] * c2 + kk[i + 2] * c1 + kk[i + 3] * c0) * (e ? bfhi(vr[i]) : bflo(vr[i]));
            __builtin_amdgcn_raw_buffer_store_b128((u32x4){pk2(o[3], o[2]), tag32, pk2(o[1], o[0]), tag32}, rs, uo + e * 8192, 0, 16);
          }
          SB();
          ST(2);
        }
        if (h == 6) {
          const u32x2 b0 = *(lds_u2*)(sm + MISC_OFF + 512 + (32 * wf + 4 * g) * 2);
          const u32x2 b1v = *(lds_u2*)(sm + MISC_OFF + 512 + (32 * wf + 4 * g) * 2 + 32);
#pragma unroll
          for (int st = 0; st < 4; ++st) {
            acc[0][st] += (f32x4){bflo(b0.x), bfhi(b0.x), bflo(b0.y), bfhi(b0.y)};
            acc[1][st] += (f32x4){bflo(b1v.x), bfhi(b1v.x), bflo(b1v.y), bfhi(b1v.y)};
          }
          SB();
        }
        if (h == 4) {
          asm volatile("" : "+v"(hlo), "+v"(hhi), "+v"(Dbits), "+v"(touch));
          if (t8 == 255) hhi = (u32x2){0, 0};
          const u32 e[6] = {hlo.x, hlo.y, hlo.z, hlo.w, hhi.x, hhi.y};
          u32 o[5];
#pragma unroll
          for (int i = 0; i < 5; ++i) o[i] = __builtin_amdgcn_alignbit(e[i + 1], e[i], 16);
          lchar* hp = hb + (t8 + 2) * 16;
          *(lds_u4*)(hp) = hlo;
          *(lds_u4*)(hp + CV_COPY) = (u32x4){o[0], o[1], o[2], o[3]};
          *(lds_u4*)(hp + 2 * CV_COPY) = (u32x4){e[1], e[2], e[3], e[4]};
          *(lds_u4*)(hp + 3 * CV_COPY) = (u32x4){o[1], o[2], o[3], o[4]};
          if ((w & 3) == 0) {
            const u32 h0 = __builtin_amdgcn_readfirstlane(hlo.x), h1 = __builtin_amdgcn_readfirstlane(hlo.y);
            const int rho = lane >> 1;
            u32x4 z = {0, 0, 0, 0};
            if (lane == 3) z.w = h0 << 16;
            if (lane == 5) z.w = h0;
            if (lane == 7) { z.z = h0 << 16; z.w = __builtin_amdgcn_alignbit(h1, h0, 16); }
            lchar* zp = lane < 8 ? hb + rho * CV_COPY + (lane & 1) * 16 : ub + 4096 + (lane & 31) * 16;
            if (lane < 8 || lane >= 32) *(lds_u4*)zp = z;
          }
          SB();
        }
      }
    }
    ST(3);
    bf16x8 af[7][4];
    const lchar* tA[4];
    {
      const int sw = (2 * g + (qq >> 1)) & 3;
#pragma unroll
      for (int T = 0; T < 4; ++T) tA[T] = sm + w * 4096 + (4 * g + qq) * 128 + ((T ^ sw) * 32) + pp * 8;
    }
    auto w2frag = [&](int k) {
      const int so = ((k + 1) % 3) * G1_SLOT;
#pragma unroll
      for (int T = 0; T < 4; ++T) af[k][T] = cat8(trread(tA[T] + so), trread(tA[T] + so + 2048));
    };
    const int cq = w & 3;
    const lchar* ap = hb + ((c + 1) & 3) * CV_COPY + ((c + 1) >> 2) * 8 + g * 16 + cq * 128;
    const lchar* bp = ub + (2048 - 16 * (c + 1) + 8 * g) * 2 + cq * 128 - 3584;
    bf16x8 cb[8], cav[8];
    auto rdA = [&](int i, int cl) {
      const u32x2 lo = *(lds_vu2*)(ap + (8 * i + cl) * 64), hi = *(lds_vu2*)(ap + (8 * i + cl) * 64 + 8);
      return __builtin_bit_cast(bf16x8, (u32x4){lo.x, lo.y, hi.x, hi.y});
    };
    auto rdB = [&](int i, int cl) { return *(lds_b8*)(bp + cl * 64 + (7 - i) * 512); };
    {
      const u32 ao = WS_ACT + ((u32)(8 * rb + 4 * wsb) * 56 + (4 * ft + wf)) * 1024 + lane * 16;
#pragma unroll
      for (int st = 0; st < 4; ++st) {
        if (st == 0) WAIT_V(8); else if (st == 1) WAIT_V(9); else if (st == 2) WAIT_V(10);
        if (st < 3) w2frag(st);
        else {
          cav[0] = rdA(0, 0);
#pragma unroll
          for (int i = 0; i < 8; ++i) cb[i] = rdB(i, 0);
          cav[1] = rdA(1, 0);
        }
        SB();
        const u32x2 m0 = mish4(acc[0][st]), m1 = mish4(acc[1][st]);
        const u32x4 r = {m0.x, m0.y, m1.x, m1.y};
        __builtin_amdgcn_raw_buffer_store_b128(r, rs, ao + st * 56 * 1024, 0, 16);
        SB();
        if (st < 3) {
          WAIT_L(0);
#pragma unroll
          for (int p = 0; p < 4; ++p) w2piece(st + 3, p);
        }
        SB();
      }
    }
    ST(4);
    bf16x8 bf[2][7];
    const u32 bo = WS_ACT + ((u32)(4 * sb) * 56 + 7 * w) * 1024 + lane * 16;
    {
      f32x4 ca[8];
#pragma unroll
      for (int n = 0; n < 8; ++n) ca[n] = (f32x4){0, 0, 0, 0};
#pragma unroll
      for (int i = 0; i < 8; ++i) {
        if (i + 2 < 8) cav[i + 2] = rdA(i + 2, 0);
        if (i == 2) { WAIT_V(11); w2frag(3); }
        SB();
#pragma unroll
        for (int n = i; n < 8; ++n) ca[n] = mfma16(cav[i], cb[n - i], ca[n]);
        SB();
        cb[7 - i] = rdB(i, 1);
        if (i >= 6) cav[i - 6] = rdA(13 - i, 1);
        if (i == 3) {
          WAIT_L(0);
#pragma unroll
          for (int p = 0; p < 4; ++p) w2piece(6, p);
        }
        SB();
      }
#pragma unroll
      for (int j = 0; j < 8; ++j) {
        const int i = 7 - j;
        if (j + 2 < 8) cav[j + 2] = rdA(5 - j, 1);
        if (j == 4) {
          WAIT_V(4);
          if (lane == 0) __hip_atomic_store(flags + FL_AF / 8 + (2 * rb + wsb) * 56 + 4 * ft + wf, tag, RLX);
          ST(5);
          w2frag(4);
        }
        if (j == 5) w2frag(5);
        if (j == 7) { WAIT_V(1); w2frag(6); }
        SB();
#pragma unroll
        for (int n = i; n < 8; ++n) ca[n] = mfma16(cav[j], cb[7 - (n - i)], ca[n]);
        SB();
      }
      ST(6);
      WAIT_L(0);
      f32x4 k0, k1;
#pragma unroll
      for (int dq = 0; dq < 4; ++dq) {
        if (dq == cq) { k0 = ca[2 * dq]; k1 = ca[2 * dq + 1]; }
        else {
          lchar* red = sm + ((cq - dq - 1) & 3) * G1_SLOT + w * 4096 + lane * 16;
          *(lds_f4*)(red) = ca[2 * dq];
          *(lds_f4*)(red + 1024) = ca[2 * dq + 1];
        }
      }
      WAIT_L(0);
      BAR();
      ST(8);
#pragma unroll
      for (int sl = 0; sl < 3; ++sl) {
        const lchar* red = sm + sl * G1_SLOT + ((w & 4) + ((cq + sl + 1) & 3)) * 4096 + lane * 16;
        k0 += *(const lds_f4*)(red);
        k1 += *(const lds_f4*)(red + 1024);
      }
      const float Dv = bflo(Dbits);
#pragma unroll
      for (int j = 0; j < 2; ++j) {
        const f32x4 m = j ? k1 : k0;
        const int t = 256 * (2 * cq + j) + 16 * c + 4 * g;
        const u32x2 uu = *(lds_u2*)(ub + (2044 - t) * 2);
        const float y0 = m[0] + bfhi(uu.y) * Dv, y1 = m[1] + bflo(uu.y) * Dv;
        const float y2 = m[2] + bfhi(uu.x) * Dv, y3 = m[3] + bflo(uu.x) * Dv;
        __builtin_amdgcn_raw_buffer_store_b128((u32x4){pk2(y0, y1), tag32, pk2(y2, y3), tag32}, rs,
                                               WS_YT + ((u32)cd * S_ + t) * 4, 0, 16);
      }
    }
    WAIT_L(0);
    BAR();
    ST(9);
      {
        gu64* afp = flags + FL_AF / 8 + sb * 56 + 7 * w + (lane < 7 ? lane : 0);
        for (u32 sp = 0; sp < SPIN_MAX; ++sp) {
          if (__all(__hip_atomic_load(afp, RLX) == tag)) break;
          __builtin_amdgcn_s_sleep(1);
        }
      }
      ST(7);
#pragma unroll
      for (int st = 0; st < 2; ++st)
#pragma unroll
        for (int k = 0; k < 7; ++k)
          bf[st][k] = __builtin_bit_cast(bf16x8, __builtin_amdgcn_raw_buffer_load_b128(rs, bo + (st * 56 + k) * 1024, 0, 16));
    const int st2 = w >> 1, th = w & 1;
    const int srow = 64 * sb + 16 * st2 + c;
    u32x4 ya, yb, q0; u32x2 gv;
    const u32 yo = WS_YT + ((u32)(64 * db + (tid >> 3)) * S_ + 64 * sb + 8 * (tid & 7)) * 4;
    SB();
#pragma unroll
    for (int st = 0; st < 4; ++st) {
      f32x4 ga[4];
#pragma unroll
      for (int T = 0; T < 4; ++T) ga[T] = (f32x4){0, 0, 0, 0};
#pragma unroll
      for (int k = 0; k < 7; ++k) {
#pragma unroll
        for (int T = 0; T < 4; ++T) ga[T] = mfma16(af[k][T], bf[st & 1][k], ga[T]);
        if (st < 2)
          bf[st & 1][k] = __builtin_bit_cast(bf16x8, __builtin_amdgcn_raw_buffer_load_b128(rs, bo + ((st + 2) * 56 + k) * 1024, 0, 16));
        SB();
      }
      if (st == 2) {
        ya = __builtin_amdgcn_raw_buffer_load_b128(rs, yo, 0, 16);
        yb = __builtin_amdgcn_raw_buffer_load_b128(rs, yo + 16, 0, 16);
        gv = __builtin_amdgcn_raw_buffer_load_b64(rs, WS_G + srow * 8, 0, 16);
        q0 = *(const u32x4*)(q + (size_t)srow * D_ + 64 * db + 32 * th + 8 * g);
      }
#pragma unroll
      for (int T = 0; T < 4; ++T) *(lds_f4*)(sm + w * 16384 + (st * 4 + T) * 1024 + lane * 16) = ga[T];
      SB();
    }
    ST(10);
    for (u32 sp = 0; sp < SPIN_MAX; ++sp) {
      if (__all(ya.y == tag32 && ya.w == tag32 && yb.y == tag32 && yb.w == tag32 && gv.y == tag32)) break;
      __builtin_amdgcn_s_sleep(1);
      ya = __builtin_amdgcn_raw_buffer_load_b128(rs, yo, 0, 16);
      yb = __builtin_amdgcn_raw_buffer_load_b128(rs, yo + 16, 0, 16);
      gv = __builtin_amdgcn_raw_buffer_load_b64(rs, WS_G + srow * 8, 0, 16);
    }
    *(lds_u4*)(sm + Y_OFF + (tid >> 3) * 144 + (tid & 7) * 16) = (u32x4){ya.x, ya.z, yb.x, yb.z};
    WAIT_L(0);
    BAR();
    ST(11);
    {
      const float gg = __uint_as_float(gv.x), og = 1.0f - gg;
      u32 res[4];
#pragma unroll
      for (int u = 0; u < 2; ++u) {
        const lchar* rp = sm + (st2 * 4 + 2 * th + (g >> 1)) * 1024 + (c + 16 * (2 * (g & 1) + u)) * 16;
        f32x4 s = *(const lds_f4*)rp;
#pragma unroll
        for (int o = 1; o < 8; ++o) s += *(const lds_f4*)(rp + o * 16384);
        const u32x2 yu = __builtin_bit_cast(u32x2, trread(sm + Y_OFF + (32 * th + 8 * g + 4 * u + qq) * 144 + (16 * st2 + 4 * pp) * 2));
        const u32 qa = q0[2 * u], qb = q0[2 * u + 1];
        const float o0 = gg * (bflo(qa) * bflo(yu.x)) + og * s[0];
        const float o1 = gg * (bfhi(qa) * bfhi(yu.x)) + og * s[1];
        const float o2 = gg * (bflo(qb) * bflo(yu.y)) + og * s[2];
        const float o3 = gg * (bfhi(qb) * bfhi(yu.y)) + og * s[3];
        res[2 * u] = pk2(o0, o1);
        res[2 * u + 1] = pk2(o2, o3);
      }
      *(u32x4*)(out + (size_t)srow * D_ + 64 * db + 32 * th + 8 * g) = (u32x4){res[0], res[1], res[2], res[3]};
    }
    ST(12);
    ST_DUMP(13);
  } else {
    const int hb = wg - 224;
    asm volatile("global_load_dwordx2 %0, %1, off sc1" : "=v"(epoch) : "v"(flags) : "memory");
    const int row0 = 64 * (4 * (hb & 7) + (hb >> 3)) + 8 * w;
    const int l56 = lane < 56 ? lane : 55;
    const u32x4 gwv = *(const u32x4*)(gw + l56 * 8);
    u32x4 xv[8];
#pragma unroll
    for (int r = 0; r < 8; ++r) xv[r] = *(const u32x4*)(q + (size_t)(row0 + r) * D_ + l56 * 8);
    WAIT_V(0);
    asm volatile("" : "+v"(epoch) :: "memory");
    const u64 tag = (((u64)(u32)__builtin_amdgcn_readfirstlane(epoch.y) << 32) | (u32)__builtin_amdgcn_readfirstlane(epoch.x)) + 0x9E3779B97F4A7C15ull;
    BAR();
    if (tid == 0) __hip_atomic_store(flags + FL_HF / 8 + hb, tag, RLX);
    {
      const char* wp = (const char*)w2 + (size_t)(hb >> 3) * (448 * 896) + (w & 1) * (224 * 896) + (w >> 1) * 7168;
      u32x4 dump = {0, 0, 0, 0};
#pragma unroll
      for (int k = 0; k < 7; ++k) {
#pragma unroll
        for (int j = 0; j < 7; ++j)
          asm volatile("global_load_dwordx4 %0, %1, %2" : "+v"(dump) : "v"(lane * 16), "s"(wp + k * 28672 + j * 1024) : "memory");
      }
      asm volatile("s_waitcnt vmcnt(0)" : "+v"(dump) :: "memory");
    }
    float zs = 0.f;
#pragma unroll
    for (int r = 0; r < 8; ++r) {
      float s = 0.f;
#pragma unroll
      for (int e = 0; e < 4; ++e) s += bflo(xv[r][e]) * bflo(gwv[e]) + bfhi(xv[r][e]) * bfhi(gwv[e]);
      if (lane >= 56) s = 0.f;
#pragma unroll
      for (int o = 32; o >= 1; o >>= 1) s += __shfl_xor(s, o);
      if (lane == r) zs = s;
    }
    if (lane < 8) {
      const float gg = 1.0f / (1.0f + __expf(-zs));
      __builtin_amdgcn_raw_buffer_store_b64((u32x2){__float_as_uint(gg), (u32)tag}, rs, WS_G + (row0 + lane) * 8, 0, 16);
    }
    ST(1);
    ST_DUMP(2);
    if (hb == 0 && w == 0) {
      for (u32 sp = 0; sp < 4 * SPIN_MAX; ++sp) {
        bool ok = __hip_atomic_load(flags + FL_HF / 8 + (lane & 31), RLX) == tag;
#pragma unroll
        for (int j = 0; j < 4; ++j) {
          const int e = lane + 64 * j, f = (e & 15) < 14 ? (e & 15) : 0;
          ok = ok && (__hip_atomic_load(flags + FL_AF / 8 + (e >> 4) * 112 + 4 * f, RLX) == tag);
        }
        if (__all(ok)) break;
        __builtin_amdgcn_s_sleep(16);
      }
      if (lane == 0) __hip_atomic_store(flags, tag, RLX);
    }
  }
}

extern "C" void kernel_launch(void* const* d_in, const int* in_sizes, int n_in, void* d_out, int out_size,
                              void* d_ws, size_t ws_size, hipStream_t stream) {
  fused_kernel<<<256, 512, 0, stream>>>((const u16*)d_in[0], (const u16*)d_in[1], (const u16*)d_in[2],
                                        (const u16*)d_in[3], (const u16*)d_in[4], (const u16*)d_in[5],
                                        (const u16*)d_in[7], (const u16*)d_in[8], (const u16*)d_in[9],
                                        (const u16*)d_in[10], (char*)d_ws, (u16*)d_out);
}
```

```cpp
#include <hip/hip_runtime.h>
#include <stdint.h>

typedef unsigned short u16;
typedef unsigned int u32;
typedef unsigned long long u64;
typedef __attribute__((ext_vector_type(8))) short bf16x8;
typedef __attribute__((ext_vector_type(4))) short s16x4;
typedef __attribute__((ext_vector_type(4))) float f32x4;
typedef __attribute__((ext_vector_type(2))) float f32x2;
typedef __attribute__((ext_vector_type(4))) u32 u32x4;
typedef __attribute__((ext_vector_type(2))) u32 u32x2;
typedef __attribute__((ext_vector_type(2))) __bf16 bf16x2_t;
typedef __attribute__((address_space(1))) u64 gu64;

constexpr int S_ = 2048, D_ = 448, F_ = 1792;
constexpr u32 WS_ACT = 0;
constexpr u32 WS_UR = 8u << 20;
constexpr u32 WS_YT = 12u << 20;
constexpr u32 WS_G = 16u << 20;
constexpr u32 WS_FLAG = WS_G + 65536;
constexpr u32 FL_HF = 1024, FL_AF = 4096;
constexpr u32 WS_END = 17u << 20;
constexpr u32 SPIN_MAX = 1u << 18;

#define WAIT_V(n) do { asm volatile("" ::: "memory"); __builtin_amdgcn_s_waitcnt(((n) & 15) | 0x0F70 | (((n) >> 4) << 14)); asm volatile("" ::: "memory"); } while (0)
#define WAIT_L(n) do { asm volatile("" ::: "memory"); __builtin_amdgcn_s_waitcnt(0xC07F | ((n) << 8)); asm volatile("" ::: "memory"); } while (0)
#define SB() __builtin_amdgcn_sched_barrier(0)
#define BAR() do { asm volatile("" ::: "memory"); __builtin_amdgcn_s_barrier(); asm volatile("" ::: "memory"); } while (0)
#define RLX __ATOMIC_RELAXED, __HIP_MEMORY_SCOPE_AGENT
#define ST(i)
#define STC(i)
#define ST_DUMP(n)

__device__ __forceinline__ float bflo(u32 x) { return __uint_as_float(x << 16); }
__device__ __forceinline__ float bfhi(u32 x) { return __uint_as_float(x & 0xffff0000u); }
__device__ __forceinline__ u32 pk2(float lo, float hi) {
  f32x2 v = {lo, hi};
  return __builtin_bit_cast(u32, __builtin_convertvector(v, bf16x2_t));
}
__device__ __forceinline__ float rbf(float x) { return bflo(pk2(x, x)); }

__device__ __forceinline__ void dma16(u32 voff, const void* sbase, u32 lds) {
  asm volatile("s_mov_b32 m0, %2\n\ts_nop 0\n\tglobal_load_lds_dwordx4 %0, %1" :: "v"(voff), "s"(sbase), "s"(lds) : "memory");
}
typedef __attribute__((address_space(3))) char lchar;
__device__ __forceinline__ s16x4 trread(const lchar* p) {
  typedef __attribute__((ext_vector_type(4))) short v4i16_t;
  return __builtin_bit_cast(s16x4, __builtin_amdgcn_ds_read_tr16_b64_v4i16(
      (__attribute__((address_space(3))) v4i16_t*)(p)));
}
__device__ __forceinline__ bf16x8 cat8(s16x4 a, s16x4 b) {
  return __builtin_shufflevector(a, b, 0, 1, 2, 3, 4, 5, 6, 7);
}
__device__ __forceinline__ f32x4 mfma16(bf16x8 a, bf16x8 b, f32x4 c) {
  return __builtin_amdgcn_mfma_f32_16x16x32_bf16(a, b, c, 0, 0, 0);
}
__device__ __forceinline__ u32x2 mish4(f32x4 h) {
  const f32x2 ha = {h[0], h[1]}, hb = {h[2], h[3]};
  const f32x2 za = ha * 1.44269504088896341f, zb = hb * 1.44269504088896341f;
  const f32x2 ta = {__builtin_amdgcn_exp2f(fminf(za[0], 15.0f)), __builtin_amdgcn_exp2f(fminf(za[1], 15.0f))};
  const f32x2 tb = {__builtin_amdgcn_exp2f(fminf(zb[0], 15.0f)), __builtin_amdgcn_exp2f(fminf(zb[1], 15.0f))};
  const f32x2 na = ta * (ta + 2.0f), nb = tb * (tb + 2.0f);
  const f32x2 da = na + 2.0f, db = nb + 2.0f;
  const f32x2 pp = da * db;
  const float R = __builtin_amdgcn_rcpf(pp[0] * pp[1]);
  const f32x2 rr = (f32x2){pp[1], pp[0]} * R;
  const f32x2 ma = ha * (na * (db * rr)), mb = hb * (nb * (da * rr));
  return (u32x2){pk2(ma[0], ma[1]), pk2(mb[0], mb[1])};
}
#define DS_B128(dst, addr, off) asm volatile("ds_read_b128 %0, %1 offset:%2" : "=v"(dst) : "v"(addr), "i"(off) : "memory")
#define DS_B64(dst, addr, off) asm volatile("ds_read_b64 %0, %1 offset:%2" : "=v"(dst) : "v"(addr), "i"(off) : "memory")
#define DS_B32(dst, addr, off) asm volatile("ds_read_b32 %0, %1 offset:%2" : "=v"(dst) : "v"(addr), "i"(off) : "memory")
#define DS_U16(dst, addr, off) asm volatile("ds_read_u16 %0, %1 offset:%2" : "=v"(dst) : "v"(addr), "i"(off) : "memory")
#define DS_TR(dst, addr, off) asm volatile("ds_read_b64_tr_b16 %0, %1 offset:%2" : "=v"(dst) : "v"(addr), "i"(off) : "memory")

constexpr int G1_SLOT = 32768;
constexpr int CV_COPY = 4160;
constexpr int CV_H = 4 * CV_COPY;
constexpr int CV_U = 2304 * 2;
constexpr int CV_CH = CV_H + CV_U;
constexpr int CV_OFF = 3 * G1_SLOT;
constexpr int PREP_BLK = 1040;
constexpr int PREP_K = CV_OFF + 2 * CV_CH, PREP_V = PREP_K + 9 * PREP_BLK, MISC_OFF = PREP_V + 8 * PREP_BLK;
constexpr int NB = 4;
constexpr int LDS_TOTAL = MISC_OFF + 1024;
constexpr int Y_OFF = PREP_K;
typedef __attribute__((address_space(3))) const volatile u32x2 lds_vu2;
typedef __attribute__((address_space(3))) const u32x2 lds_u2;
typedef __attribute__((address_space(3))) u32x4 lds_u4;
typedef __attribute__((address_space(3))) f32x4 lds_f4;
typedef __attribute__((address_space(3))) const bf16x8 lds_b8;

__global__ __launch_bounds__(512, 2) void fused_kernel(const u16* __restrict__ q, const u16* __restrict__ kin,
    const u16* __restrict__ vin, const u16* __restrict__ sk, const u16* __restrict__ ssmk,
    const u16* __restrict__ ssmD, const u16* __restrict__ w1, const u16* __restrict__ b1,
    const u16* __restrict__ w2, const u16* __restrict__ gw, char* __restrict__ ws, u16* __restrict__ out) {
  __shared__ __attribute__((aligned(16))) char smraw[LDS_TOTAL];
  lchar* sm = (lchar*)smraw;
  const int tid = threadIdx.x, lane = tid & 63;
  const int w = __builtin_amdgcn_readfirstlane(tid >> 6);
  const int wg = blockIdx.x;
  ST(0); STC(14);
  const auto rs = __builtin_amdgcn_make_buffer_rsrc(ws, 0, WS_END, 0x00020000);
  gu64* flags = (gu64*)(ws + WS_FLAG);
  const u32 lbase = (u32)(uintptr_t)sm;
  u32x2 epoch;

  if (wg < 224) {
    const int xcd = wg & 7, idx = wg >> 3;
    const int i7 = idx / 7, m7 = idx - 7 * i7;
    const int rb = 4 * (xcd >> 1) + i7, ft = 7 * (xcd & 1) + m7;
    const int sb = 4 * xcd + i7, db = m7;
    const int cd = 2 * wg + (w >> 2);
    const int t8 = tid & 255;
    const u32 xo0 = (lane >> 3) * (D_ * 2) + ((lane & 7) ^ (lane >> 4)) * 16;
    const u32 xo1 = ((lane >> 3) + 8) * (D_ * 2) + ((lane & 7) ^ (lane >> 4) ^ 4) * 16;
    const u32 wo0 = (lane >> 4) * (F_ * 2) + ((lane & 15) ^ (2 * (lane >> 4)) ^ (8 * (w & 1))) * 16;
    const u16* xs = q + (size_t)(128 * rb + 16 * w) * D_;
    const u16* wsr = w1 + (size_t)(8 * w) * F_ + 128 * ft;
    auto stage = [&](int t, int p) {
      const u32 slot = lbase + (t % 3) * G1_SLOT + w * 2048;
      if (p == 0) dma16(xo0, xs + 64 * t, slot);
      if (p == 1) dma16(xo1, xs + 64 * t, slot + 1024);
      if (p == 2) dma16(wo0, wsr + (size_t)(64 * t) * F_, slot + 16384);
      if (p == 3) dma16(wo0, wsr + (size_t)(64 * t + 4) * F_, slot + 16384 + 1024);
    };
    const u32 w2o = (lane >> 3) * (D_ * 2) + ((lane & 7) ^ (2 * (lane >> 4))) * 16;
    const u16* w2s = w2 + (size_t)(224 * w) * D_ + 64 * db;
    auto w2piece = [&](int k, int p) {
      dma16(w2o, w2s + (size_t)(32 * k + 8 * p) * D_, lbase + ((k + 1) % 3) * G1_SLOT + w * 4096 + p * 1024);
    };
#pragma unroll
    for (int p = 0; p < 4; ++p) stage(0, p);
    asm volatile("global_load_dwordx2 %0, %1, off sc1" : "=v"(epoch) : "v"(flags) : "memory");
    {
      const u32 ko = (lane >> 3) * (D_ * 2) + (lane & 7) * 16;
      const size_t e0 = (size_t)(64 * sb + 8 * w) * D_ + 64 * db;
      dma16(ko, kin + e0, lbase + PREP_K + (w + 1) * PREP_BLK);
      dma16(ko, vin + e0, lbase + PREP_V + w * PREP_BLK);
      if (w == 0) dma16(ko, kin + (size_t)(sb ? 64 * sb - 8 : 0) * D_ + 64 * db, lbase + PREP_K);
      if (w == 1) {
        const u16* p = (lane < 32) ? (sk + 256 * db + lane * 8) : (b1 + 128 * ft + (lane & 15) * 8);
        asm volatile("s_mov_b32 m0, %1\n\ts_nop 0\n\tglobal_load_lds_dwordx4 %0, off" :: "v"(p), "s"(lbase + MISC_OFF) : "memory");
      }
    }
#pragma unroll
    for (int p = 0; p < 4; ++p) stage(1, p);
    u32 touch;
    {
      int xi = m7 * 147 + (t8 < 147 ? t8 : 146); xi = xi < 1024 ? xi : 1023;
      const u32 to = (w < 4) ? (u32)(192 + 64 * i7 + (t8 >> 2)) * (F_ * 2) + ft * 256 + (t8 & 3) * 64
                             : (u32)(128 * rb + (xi >> 3)) * (D_ * 2) + 384 + (xi & 7) * 64;
      asm volatile("global_load_dword %0, %1, %2" : "=v"(touch) : "v"(to), "s"(w < 4 ? w1 : q) : "memory");
    }
    u32x4 hlo; u32x2 hhi; u32 Dbits;
    const u16* hs = ssmk + (size_t)cd * S_;

    const int wf = w & 3, wsb = w >> 2;
    const int c = lane & 15, g = lane >> 4, qq = (lane >> 2) & 3, pp = lane & 3;
    lchar* hb = sm + CV_OFF + (w >> 2) * CV_CH;
    lchar* ub = hb + CV_H;
    const u32 uoff = WS_UR + (u32)cd * 8192 + t8 * 32;
    u32x4 ua, ub4;
    u64 tag; u32 tag32;
    f32x4 acc[2][4];
#pragma unroll
    for (int T = 0; T < 2; ++T)
#pragma unroll
      for (int st = 0; st < 4; ++st) acc[T][st] = (f32x4){0, 0, 0, 0};
    {
      const u32 boff = lbase + (64 * wsb + c) * 128 + ((g ^ (c >> 1)) * 16);
      const int hh = qq | ((g & 1) << 2);
      const u32 aoff = lbase + 16384 + (8 * g + qq) * 256 + (((4 * wf + (pp >> 1)) ^ (2 * hh)) * 16) + 8 * (pp & 1);
      const u32 bA[2][2] = {{boff, boff ^ 64}, {boff + 65536, (boff ^ 64) + 65536}};
      const u32 aA[2][2] = {{aoff, aoff ^ 32}, {aoff + 65536, (aoff ^ 32) + 65536}};
      bf16x8 bfr[2][4]; s16x4 afr[2][2][2];
      auto rd = [&](int h) {
        const int t = h >> 1, ks = h & 1, sl = t % 3, hi = sl == 2, so = (sl == 1) * G1_SLOT;
#pragma unroll
        for (int T = 0; T < 2; ++T) {
          DS_TR(afr[ks][T][0], aA[hi][T], so + ks * 8192);
          DS_TR(afr[ks][T][1], aA[hi][T], so + ks * 8192 + 1024);
        }
#pragma unroll
        for (int st = 0; st < 4; ++st) DS_B128(bfr[ks][st], bA[hi][ks], so + st * 2048);
      };
      WAIT_V(8);
      BAR();
      ST(1);
      rd(0);
#pragma unroll
      for (int h = 0; h < 14; ++h) {
        const int t = h >> 1, ks = h & 1;
        if (ks == 1) {
          if (h == 13) {
            asm volatile("s_waitcnt vmcnt(8)" : "+v"(ua), "+v"(ub4) :: "memory");
            for (u32 sp = 0; sp < SPIN_MAX; ++sp) {
              if (__all(ua.y == tag32 && ua.w == tag32 && ub4.y == tag32 && ub4.w == tag32)) break;
              __builtin_amdgcn_s_sleep(2);
              asm volatile("buffer_load_dwordx4 %0, %2, %3, 0 offen sc1\n\tbuffer_load_dwordx4 %1, %2, %3, 0 offen offset:16 sc1\n\ts_waitcnt vmcnt(0)"
                           : "=&v"(ua), "=&v"(ub4) : "v"(uoff), "s"(rs) : "memory");
            }
            *(lds_u4*)(ub + t8 * 16) = (u32x4){ua.x, ua.z, ub4.x, ub4.z};
          }
          asm volatile("s_waitcnt lgkmcnt(0)" : "+v"(afr[1][0][0]), "+v"(afr[1][0][1]), "+v"(afr[1][1][0]), "+v"(afr[1][1][1]),
                       "+v"(bfr[1][0]), "+v"(bfr[1][1]), "+v"(bfr[1][2]), "+v"(bfr[1][3]) :: "memory");
          if (h == 1) WAIT_V(8); else if (h == 3 || h == 5 || h == 11) WAIT_V(6); else if (h == 7 || h == 9) WAIT_V(4);
          BAR();
          if (h < 13) rd(h + 1);
          if (h == 9)
            asm volatile("buffer_load_dwordx4 %0, %2, %3, 0 offen sc1\n\tbuffer_load_dwordx4 %1, %2, %3, 0 offen offset:16 sc1"
                         : "=&v"(ua), "=&v"(ub4) : "v"(uoff), "s"(rs) : "memory");
        } else {
          rd(h + 1);
          asm volatile("s_waitcnt lgkmcnt(8)" : "+v"(afr[0][0][0]), "+v"(afr[0][0][1]), "+v"(afr[0][1][0]), "+v"(afr[0][1][1]),
                       "+v"(bfr[0][0]), "+v"(bfr[0][1]), "+v"(bfr[0][2]), "+v"(bfr[0][3]) :: "memory");
        }
        SB();
#pragma unroll
        for (int T = 0; T < 2; ++T) {
          const bf16x8 a = cat8(afr[ks][T][0], afr[ks][T][1]);
#pragma unroll
          for (int st = 0; st < 4; ++st) {
            acc[T][st] = mfma16(a, bfr[ks][st], acc[T][st]);
            if (ks == 1 && (st & 1) == 0) {
              SB();
              const int p = 2 * T + (st >> 1);
              if (t + 3 < 7) stage(t + 3, p); else w2piece(t - 4, p);
              SB();
            }
            if (h == 0) {
              SB();
              if (T == 0) stage(2, st);
              else if (st == 0) asm volatile("global_load_dwordx4 %0, %1, %2" : "=v"(hlo) : "v"(t8 * 16), "s"(hs) : "memory");
              else if (st == 1) asm volatile("global_load_dwordx2 %0, %1, %2" : "=v"(hhi) : "v"(t8 < 255 ? t8 * 16 + 16 : 0), "s"(hs) : "memory");
              else if (st == 2) asm volatile("global_load_ushort %0, %1, %2" : "=v"(Dbits) : "v"(0), "s"(ssmD + cd) : "memory");
              SB();
            }
          }
        }
        SB();
        if (h == 2) {
          asm volatile("" : "+v"(epoch));
          tag = (((u64)(u32)__builtin_amdgcn_readfirstlane(epoch.y) << 32) | (u32)__builtin_amdgcn_readfirstlane(epoch.x)) + 0x9E3779B97F4A7C15ull;
          tag32 = (u32)tag;
          const int r4 = tid & 15, dd = tid >> 4;
          const u32 kb = lbase + PREP_K + (1 + (r4 >> 1)) * PREP_BLK + (r4 & 1) * 512 + dd * 4;
          const u32 ka = kb - ((r4 & 1) ? 384 : 400);
          u32 kr[7], vr[4]; u32x4 skv;
#pragma unroll
          for (int i = 0; i < 3; ++i) DS_B32(kr[i], ka, i * 128);
#pragma unroll
          for (int i = 0; i < 4; ++i) DS_B32(kr[3 + i], kb, i * 128);
#pragma unroll
          for (int i = 0; i < 4; ++i) DS_B32(vr[i], kb, (PREP_V - PREP_K - PREP_BLK) + i * 128);
          const u32 sa = lbase + MISC_OFF + dd * 16;
          DS_B128(skv, sa, 0);
          asm volatile("s_waitcnt lgkmcnt(0)" : "+v"(kr[0]), "+v"(kr[1]), "+v"(kr[2]), "+v"(kr[3]), "+v"(kr[4]), "+v"(kr[5]),
                       "+v"(kr[6]), "+v"(vr[0]), "+v"(vr[1]), "+v"(vr[2]), "+v"(vr[3]), "+v"(skv) :: "memory");
          if (sb == 0 && r4 == 0) { kr[0] = 0; kr[1] = 0; kr[2] = 0; }
          const u32 uo = WS_UR + ((u32)(64 * db + 2 * dd) * S_ + (2044 - 64 * sb - 4 * r4)) * 4;
#pragma unroll
          for (int e = 0; e < 2; ++e) {
            const float c0 = bflo(skv[2 * e]), c1 = bfhi(skv[2 * e]), c2 = bflo(skv[2 * e + 1]), c3 = bfhi(skv[2 * e + 1]);
            float kk[7], o[4];
#pragma unroll
            for (int i = 0; i < 7; ++i) kk[i] = e ? bfhi(kr[i]) : bflo(kr[i]);
#pragma unroll
            for (int i = 0; i < 4; ++i)
              o[i] = (kk[i] * c3 + kk[i + 1] * c2 + kk[i + 2] * c1 + kk[i + 3] * c0) * (e ? bfhi(vr[i]) : bflo(vr[i]));
            __builtin_amdgcn_raw_buffer_store_b128((u32x4){pk2(o[3], o[2]), tag32, pk2(o[1], o[0]), tag32}, rs, uo + e * 8192, 0, 16);
          }
          SB();
          ST(2);
        }
        if (h == 6) {
          const u32x2 b0 = *(lds_u2*)(sm + MISC_OFF + 512 + (32 * wf + 4 * g) * 2);
          const u32x2 b1v = *(lds_u2*)(sm + MISC_OFF + 512 + (32 * wf + 4 * g) * 2 + 32);
#pragma unroll
          for (int st = 0; st < 4; ++st) {
            acc[0][st] += (f32x4){bflo(b0.x), bfhi(b0.x), bflo(b0.y), bfhi(b0.y)};
            acc[1][st] += (f32x4){bflo(b1v.x), bfhi(b1v.x), bflo(b1v.y), bfhi(b1v.y)};
          }
          SB();
        }
        if (h == 4) {
          asm volatile("" : "+v"(hlo), "+v"(hhi), "+v"(Dbits), "+v"(touch));
          if (t8 == 255) hhi = (u32x2){0, 0};
          const u32 e[6] = {hlo.x, hlo.y, hlo.z, hlo.w, hhi.x, hhi.y};
          u32 o[5];
#pragma unroll
          for (int i = 0; i < 5; ++i) o[i] = __builtin_amdgcn_alignbit(e[i + 1], e[i], 16);
          lchar* hp = hb + (t8 + 2) * 16;
          *(lds_u4*)(hp) = hlo;
          *(lds_u4*)(hp + CV_COPY) = (u32x4){o[0], o[1], o[2], o[3]};
          *(lds_u4*)(hp + 2 * CV_COPY) = (u32x4){e[1], e[2], e[3], e[4]};
          *(lds_u4*)(hp + 3 * CV_COPY) = (u32x4){o[1], o[2], o[3], o[4]};
          if ((w & 3) == 0) {
            const u32 h0 = __builtin_amdgcn_readfirstlane(hlo.x), h1 = __builtin_amdgcn_readfirstlane(hlo.y);
            const int rho = lane >> 1;
            u32x4 z = {0, 0, 0, 0};
            if (lane == 3) z.w = h0 << 16;
            if (lane == 5) z.w = h0;
            if (lane == 7) { z.z = h0 << 16; z.w = __builtin_amdgcn_alignbit(h1, h0, 16); }
            lchar* zp = lane < 8 ? hb + rho * CV_COPY + (lane & 1) * 16 : ub + 4096 + (lane & 31) * 16;
            if (lane < 8 || lane >= 32) *(lds_u4*)zp = z;
          }
          SB();
        }
      }
    }
    ST(3);
    bf16x8 af[7][4];
    const lchar* tA[4];
    {
      const int sw = (2 * g + (qq >> 1)) & 3;
#pragma unroll
      for (int T = 0; T < 4; ++T) tA[T] = sm + w * 4096 + (4 * g + qq) * 128 + ((T ^ sw) * 32) + pp * 8;
    }
    auto w2frag = [&](int k) {
      const int so = ((k + 1) % 3) * G1_SLOT;
#pragma unroll
      for (int T = 0; T < 4; ++T) af[k][T] = cat8(trread(tA[T] + so), trread(tA[T] + so + 2048));
    };
    const int cq = w & 3;
    const lchar* ap = hb + ((c + 1) & 3) * CV_COPY + ((c + 1) >> 2) * 8 + g * 16 + cq * 128;
    const lchar* bp = ub + (2048 - 16 * (c + 1) + 8 * g) * 2 + cq * 128 - 3584;
    bf16x8 cb[8], cav[8];
    auto rdA = [&](int i, int cl) {
      const u32x2 lo = *(lds_vu2*)(ap + (8 * i + cl) * 64), hi = *(lds_vu2*)(ap + (8 * i + cl) * 64 + 8);
      return __builtin_bit_cast(bf16x8, (u32x4){lo.x, lo.y, hi.x, hi.y});
    };
    auto rdB = [&](int i, int cl) { return *(lds_b8*)(bp + cl * 64 + (7 - i) * 512); };
    {
      const u32 ao = WS_ACT + ((u32)(8 * rb + 4 * wsb) * 56 + (4 * ft + wf)) * 1024 + lane * 16;
#pragma unroll
      for (int st = 0; st < 4; ++st) {
        if (st == 0) WAIT_V(8); else if (st == 1) WAIT_V(5); else if (st == 2) WAIT_V(2);
        if (st < 3) w2frag(st);
        else {
          cav[0] = rdA(0, 0);
#pragma unroll
          for (int i = 0; i < 8; ++i) cb[i] = rdB(i, 0);
          cav[1] = rdA(1, 0);
        }
        SB();
        const u32x2 m0 = mish4(acc[0][st]), m1 = mish4(acc[1][st]);
        const u32x4 r = {m0.x, m0.y, m1.x, m1.y};
        __builtin_amdgcn_raw_buffer_store_b128(r, rs, ao + st * 56 * 1024, 0, 16);
        SB();
      }
      WAIT_L(0);
    }
    ST(4);
    bf16x8 bf[NB][7];
    gu64* afp = flags + FL_AF / 8 + sb * 56 + 7 * w + (lane < 7 ? lane : 0);
    u64 fl0;
    const u32 bo = WS_ACT + ((u32)(4 * sb) * 56 + 7 * w) * 1024 + lane * 16;
    {
      f32x4 ca[8];
#pragma unroll
      for (int n = 0; n < 8; ++n) ca[n] = (f32x4){0, 0, 0, 0};
#pragma unroll
      for (int i = 0; i < 8; ++i) {
        if (i + 2 < 8) cav[i + 2] = rdA(i + 2, 0);
        SB();
#pragma unroll
        for (int n = i; n < 8; ++n) ca[n] = mfma16(cav[i], cb[n - i], ca[n]);
        SB();
        cb[7 - i] = rdB(i, 1);
        if (i >= 6) cav[i - 6] = rdA(13 - i, 1);
        if (i < 6) { w2piece(3 + (i >> 1), 2 * (i & 1)); w2piece(3 + (i >> 1), 2 * (i & 1) + 1); }
        if (i == 7) {
          WAIT_V(12);
          if (lane == 0) __hip_atomic_store(flags + FL_AF / 8 + (2 * rb + wsb) * 56 + 4 * ft + wf, tag, RLX);
          ST(5);
        }
        SB();
      }
#pragma unroll
      for (int j = 0; j < 8; ++j) {
        const int i = 7 - j;
        if (j + 2 < 8) cav[j + 2] = rdA(5 - j, 1);
        if (j == 0) { WAIT_V(9); w2frag(3); }
        if (j == 2) {
          WAIT_L(0);
#pragma unroll
          for (int p = 0; p < 4; ++p) w2piece(6, p);
        }
        if (j == 3) { WAIT_V(9); w2frag(4); }
        if (j == 5) { WAIT_V(5); w2frag(5); }
        SB();
#pragma unroll
        for (int n = i; n < 8; ++n) ca[n] = mfma16(cav[j], cb[7 - (n - i)], ca[n]);
        SB();
      }
      ST(6);
      WAIT_L(0);
      f32x4 k0, k1;
#pragma unroll
      for (int dq = 0; dq < 4; ++dq) {
        if (dq == cq) { k0 = ca[2 * dq]; k1 = ca[2 * dq + 1]; }
        else {
          const int ri = (cq - dq - 1) & 3;
          lchar* red = sm + (ri & 2) * G1_SLOT + (ri & 1) * 2048 + w * 4096 + lane * 16;
          *(lds_f4*)(red) = ca[2 * dq];
          *(lds_f4*)(red + 1024) = ca[2 * dq + 1];
        }
      }
      WAIT_L(0);
      BAR();
      ST(8);
      fl0 = __hip_atomic_load(afp, RLX);
      SB();
#pragma unroll
      for (int sl = 0; sl < 3; ++sl) {
        const lchar* red = sm + (sl & 2) * G1_SLOT + (sl & 1) * 2048 + ((w & 4) + ((cq + sl + 1) & 3)) * 4096 + lane * 16;
        k0 += *(const lds_f4*)(red);
        k1 += *(const lds_f4*)(red + 1024);
      }
      const float Dv = bflo(Dbits);
#pragma unroll
      for (int j = 0; j < 2; ++j) {
        const f32x4 m = j ? k1 : k0;
        const int t = 256 * (2 * cq + j) + 16 * c + 4 * g;
        const u32x2 uu = *(lds_u2*)(ub + (2044 - t) * 2);
        const float y0 = m[0] + bfhi(uu.y) * Dv, y1 = m[1] + bflo(uu.y) * Dv;
        const float y2 = m[2] + bfhi(uu.x) * Dv, y3 = m[3] + bflo(uu.x) * Dv;
        __builtin_amdgcn_raw_buffer_store_b128((u32x4){pk2(y0, y1), tag32, pk2(y2, y3), tag32}, rs,
                                               WS_YT + ((u32)cd * S_ + t) * 4, 0, 16);
      }
    }
    SB();
    WAIT_V(2);
    const bool ok0 = __all(fl0 == tag);
    w2frag(6);
    WAIT_L(0);
    BAR();
    ST(9);
      {
        if (!ok0)
          for (u32 sp = 0; sp < SPIN_MAX; ++sp) {
            if (__all(__hip_atomic_load(afp, RLX) == tag)) break;
            __builtin_amdgcn_s_sleep(1);
          }
      }
      ST(7);
#pragma unroll
      for (int st = 0; st < NB; ++st)
#pragma unroll
        for (int k = 0; k < 7; ++k)
          bf[st][k] = __builtin_bit_cast(bf16x8, __builtin_amdgcn_raw_buffer_load_b128(rs, bo + (st * 56 + k) * 1024, 0, 16));
    const int st2 = w >> 1, th = w & 1;
    const int srow = 64 * sb + 16 * st2 + c;
    u32x4 ya, yb, q0; u32x2 gv;
    const u32 yo = WS_YT + ((u32)(64 * db + (tid >> 3)) * S_ + 64 * sb + 8 * (tid & 7)) * 4;
    SB();
#pragma unroll
    for (int st = 0; st < 4; ++st) {
      f32x4 ga[4];
#pragma unroll
      for (int T = 0; T < 4; ++T) ga[T] = (f32x4){0, 0, 0, 0};
#pragma unroll
      for (int k = 0; k < 7; ++k) {
#pragma unroll
        for (int T = 0; T < 4; ++T) ga[T] = mfma16(af[k][T], bf[st % NB][k], ga[T]);
        if (st + NB < 4)
          bf[st % NB][k] = __builtin_bit_cast(bf16x8, __builtin_amdgcn_raw_buffer_load_b128(rs, bo + ((st + NB) * 56 + k) * 1024, 0, 16));
        SB();
      }
      if (st == 1) {
        ya = __builtin_amdgcn_raw_buffer_load_b128(rs, yo, 0, 16);
        yb = __builtin_amdgcn_raw_buffer_load_b128(rs, yo + 16, 0, 16);
        gv = __builtin_amdgcn_raw_buffer_load_b64(rs, WS_G + srow * 8, 0, 16);
        q0 = *(const u32x4*)(q + (size_t)srow * D_ + 64 * db + 32 * th + 8 * g);
      }
#pragma unroll
      for (int T = 0; T < 4; ++T) *(lds_f4*)(sm + w * 16384 + (st * 4 + T) * 1024 + lane * 16) = ga[T];
      SB();
    }
    ST(10);
    for (u32 sp = 0; sp < SPIN_MAX; ++sp) {
      if (__all(ya.y == tag32 && ya.w == tag32 && yb.y == tag32 && yb.w == tag32 && gv.y == tag32)) break;
      __builtin_amdgcn_s_sleep(1);
      ya = __builtin_amdgcn_raw_buffer_load_b128(rs, yo, 0, 16);
      yb = __builtin_amdgcn_raw_buffer_load_b128(rs, yo + 16, 0, 16);
      gv = __builtin_amdgcn_raw_buffer_load_b64(rs, WS_G + srow * 8, 0, 16);
    }
    *(lds_u4*)(sm + Y_OFF + (tid >> 3) * 144 + (tid & 7) * 16) = (u32x4){ya.x, ya.z, yb.x, yb.z};
    WAIT_L(0);
    BAR();
    ST(11);
    {
      const float gg = __uint_as_float(gv.x), og = 1.0f - gg;
      u32 res[4];
#pragma unroll
      for (int u = 0; u < 2; ++u) {
        const lchar* rp = sm + (st2 * 4 + 2 * th + (g >> 1)) * 1024 + (c + 16 * (2 * (g & 1) + u)) * 16;
        f32x4 s = *(const lds_f4*)rp;
#pragma unroll
        for (int o = 1; o < 8; ++o) s += *(const lds_f4*)(rp + o * 16384);
        const u32x2 yu = __builtin_bit_cast(u32x2, trread(sm + Y_OFF + (32 * th + 8 * g + 4 * u + qq) * 144 + (16 * st2 + 4 * pp) * 2));
        const u32 qa = q0[2 * u], qb = q0[2 * u + 1];
        const float o0 = gg * (bflo(qa) * bflo(yu.x)) + og * s[0];
        const float o1 = gg * (bfhi(qa) * bfhi(yu.x)) + og * s[1];
        const float o2 = gg * (bflo(qb) * bflo(yu.y)) + og * s[2];
        const float o3 = gg * (bfhi(qb) * bfhi(yu.y)) + og * s[3];
        res[2 * u] = pk2(o0, o1);
        res[2 * u + 1] = pk2(o2, o3);
      }
      *(u32x4*)(out + (size_t)srow * D_ + 64 * db + 32 * th + 8 * g) = (u32x4){res[0], res[1], res[2], res[3]};
    }
    ST(12);
    ST_DUMP(13);
  } else {
    const int hb = wg - 224;
    asm volatile("global_load_dwordx2 %0, %1, off sc1" : "=v"(epoch) : "v"(flags) : "memory");
    const int row0 = 64 * (4 * (hb & 7) + (hb >> 3)) + 8 * w;
    const int l56 = lane < 56 ? lane : 55;
    const u32x4 gwv = *(const u32x4*)(gw + l56 * 8);
    u32x4 xv[8];
#pragma unroll
    for (int r = 0; r < 8; ++r) xv[r] = *(const u32x4*)(q + (size_t)(row0 + r) * D_ + l56 * 8);
    WAIT_V(0);
    asm volatile("" : "+v"(epoch) :: "memory");
    const u64 tag = (((u64)(u32)__builtin_amdgcn_readfirstlane(epoch.y) << 32) | (u32)__builtin_amdgcn_readfirstlane(epoch.x)) + 0x9E3779B97F4A7C15ull;
    BAR();
    if (tid == 0) __hip_atomic_store(flags + FL_HF / 8 + hb, tag, RLX);
    {
      const char* wp = (const char*)w2 + (size_t)(hb >> 3) * (448 * 896) + (w & 1) * (224 * 896) + (w >> 1) * 7168;
      u32x4 dump = {0, 0, 0, 0};
#pragma unroll
      for (int k = 0; k < 7; ++k) {
#pragma unroll
        for (int j = 0; j < 7; ++j)
          asm volatile("global_load_dwordx4 %0, %1, %2" : "+v"(dump) : "v"(lane * 16), "s"(wp + k * 28672 + j * 1024) : "memory");
      }
      asm volatile("s_waitcnt vmcnt(0)" : "+v"(dump) :: "memory");
    }
    float zs = 0.f;
#pragma unroll
    for (int r = 0; r < 8; ++r) {
      float s = 0.f;
#pragma unroll
      for (int e = 0; e < 4; ++e) s += bflo(xv[r][e]) * bflo(gwv[e]) + bfhi(xv[r][e]) * bfhi(gwv[e]);
      if (lane >= 56) s = 0.f;
#pragma unroll
      for (int o = 32; o >= 1; o >>= 1) s += __shfl_xor(s, o);
      if (lane == r) zs = s;
    }
    if (lane < 8) {
      const float gg = 1.0f / (1.0f + __expf(-zs));
      __builtin_amdgcn_raw_buffer_store_b64((u32x2){__float_as_uint(gg), (u32)tag}, rs, WS_G + (row0 + lane) * 8, 0, 16);
    }
    ST(1);
    ST_DUMP(2);
    if (hb == 0 && w == 0) {
      for (u32 sp = 0; sp < 4 * SPIN_MAX; ++sp) {
        bool ok = __hip_atomic_load(flags + FL_HF / 8 + (lane & 31), RLX) == tag;
#pragma unroll
        for (int j = 0; j < 4; ++j) {
          const int e = lane + 64 * j, f = (e & 15) < 14 ? (e & 15) : 0;
          ok = ok && (__hip_atomic_load(flags + FL_AF / 8 + (e >> 4) * 112 + 4 * f, RLX) == tag);
        }
        if (__all(ok)) break;
        __builtin_amdgcn_s_sleep(16);
      }
      if (lane == 0) __hip_atomic_store(flags, tag, RLX);
    }
  }
}

extern "C" void kernel_launch(void* const* d_in, const int* in_sizes, int n_in, void* d_out, int out_size,
                              void* d_ws, size_t ws_size, hipStream_t stream) {
  fused_kernel<<<256, 512, 0, stream>>>((const u16*)d_in[0], (const u16*)d_in[1], (const u16*)d_in[2],
                                        (const u16*)d_in[3], (const u16*)d_in[4], (const u16*)d_in[5],
                                        (const u16*)d_in[7], (const u16*)d_in[8], (const u16*)d_in[9],
                                        (const u16*)d_in[10], (char*)d_ws, (u16*)d_out);
}
```
